```python
import torch

def _softmax(x, axis=-1):
    return torch.softmax(x, dim=axis)
def _gelu(x):
    return torch.nn.functional.gelu(x, approximate="tanh")
def _silu(x):
    return torch.nn.functional.silu(x)
def _sigmoid(x):
    return torch.sigmoid(x)
def _layernorm(x, axis=-1, eps=1e-5):
    m = x.mean(dim=axis, keepdim=True)
    v = x.var(dim=axis, keepdim=True, unbiased=False)
    return (x - m) / torch.sqrt(v + eps)
def _scatter_add(tgt, idx, src, axis=0):
    return tgt.clone().index_add_(axis, idx, src)
def _segment_sum(src, seg, n):
    out = torch.zeros((n,) + src.shape[1:], dtype=src.dtype)
    return out.index_add_(0, seg, src)
def _pad(x, pw):
    flat = tuple(v for pair in reversed(pw) for v in pair)
    return torch.nn.functional.pad(x, flat)

kernel_name = 'gated_mix__linear_attn_block__glu_gated_glu__prompt'

def setup_inputs():

    _w = (lambda a: (lambda t: t.to(torch.bfloat16) if t.is_floating_point() else t)(torch.as_tensor(a)))
    def _A_setup():
        B, S, D = 1, 2048, 640
        return {
            "x": _w((lambda *s: torch.randn(*s, dtype=torch.bfloat16) * 0.05)(B, S, D)),
            "wqkv": _w((lambda *s: torch.randn(*s, dtype=torch.bfloat16) * 0.8)(D, 3 * D)),
            "wo": _w((lambda *s: torch.randn(*s, dtype=torch.bfloat16) * 0.2)(D, D)),
        }
    _A_in = _A_setup()

    def _B_setup():
        B, S, D, F = 1, 2048, 640, 1536
        return {
            "x": _w((lambda *s: torch.randn(*s, dtype=torch.bfloat16) * 0.05)(B, S, D)),
            "w_gate": _w((lambda *s: torch.randn(*s, dtype=torch.bfloat16) * 0.4)(D, F)),
            "w_up": _w((lambda *s: torch.randn(*s, dtype=torch.bfloat16) * 0.05)(D, F)),
            "w_down": _w((lambda *s: torch.randn(*s, dtype=torch.bfloat16) * 0.05)(F, D)),
        }
    _B_in = _B_setup()

    return {
        "A_x": _A_in["x"],
        "A_wqkv": _A_in["wqkv"],
        "A_wo": _A_in["wo"],
        "B_x": _B_in["x"],
        "B_w_gate": _B_in["w_gate"],
        "B_w_up": _B_in["w_up"],
        "B_w_down": _B_in["w_down"],
        "gate_w": _w((lambda *s: torch.randn(*s, dtype=torch.bfloat16) * 0.05)(640) * 16.0),
    }

def reference(A_x, A_wqkv, A_wo, B_x, B_w_gate, B_w_up, B_w_down, gate_w):
    def _A_ref(x, wqkv, wo):
        B, S, D = x.shape
        H = 32; Dh = D // H
        qkv = (lambda x, *d: x.permute(*d))((x @ wqkv).reshape(B, S, 3, H, Dh), 2, 0, 3, 1, 4)
        q, k, v = qkv[0], qkv[1], qkv[2]
        phi = lambda t: torch.where(t > 0, t + 1, torch.exp(t)) + 1e-6
        pq, pk = phi(q), phi(k)
        kv = torch.einsum("bhsd,bhse->bhde", pk, v)
        z = torch.einsum("bhsd->bhd", pk)
        num = torch.einsum("bhqd,bhde->bhqe", pq, kv)
        den = torch.einsum("bhqd,bhd->bhq", pq, z)[..., None] + 1e-6
        o = num / den
        return (lambda x, *d: x.permute(*d))(o, 0, 2, 1, 3).reshape(B, S, D) @ wo

    def _B_ref(x, w_gate, w_up, w_down):
        g = torch.einsum("bsd,df->bsf", x, w_gate)
        u = torch.einsum("bsd,df->bsf", x, w_up)
        h = _sigmoid(g) * u
        return torch.einsum("bsf,fd->bsd", h, w_down)

    x = A_x
    g = _sigmoid((x * gate_w).sum(-1, keepdims=True))
    a = _A_ref(x, A_wqkv, A_wo)
    b = _B_ref(x, B_w_gate, B_w_up, B_w_down)
    return g * a + (1 - g) * b
```

```cpp
#include <hip/hip_runtime.h>
#include <hip/hip_bf16.h>
typedef unsigned short u16;
typedef short bf16x8 __attribute__((ext_vector_type(8)));
typedef float f32x4 __attribute__((ext_vector_type(4)));

__device__ __forceinline__ float bf2f(u16 v){ return __uint_as_float(((unsigned)v)<<16); }
__device__ __forceinline__ u16 f2bf(float f){ __hip_bfloat16 b = __float2bfloat16(f); return *(u16*)&b; }
__device__ __forceinline__ float rb(float f){ return bf2f(f2bf(f)); }

__global__ __launch_bounds__(256) void gemm_simple(const u16* A, int lda, const u16* B, int ldb, u16* C, int ldc, int K){
  __shared__ u16 sA[64][40];
  __shared__ u16 sB[32][72];
  int bm = blockIdx.y*64, bn = blockIdx.x*64;
  int tid = threadIdx.x, wid = tid>>6, lane = tid&63;
  int wr = wid>>1, wc = wid&1;
  f32x4 acc[2][2] = {};
  for(int k0=0;k0<K;k0+=32){
    { int r = tid>>2, c = (tid&3)*8;
      for(int j=0;j<8;++j) sA[r][c+j] = A[(long)(bm+r)*lda + k0+c+j]; }
    { int r = tid>>3, c = (tid&7)*8;
      for(int j=0;j<8;++j) sB[r][c+j] = B[(long)(k0+r)*ldb + bn+c+j]; }
    __syncthreads();
    bf16x8 a[2], b[2];
    for(int m=0;m<2;++m) for(int j=0;j<8;++j) a[m][j] = sA[wr*32+m*16+(lane&15)][(lane>>4)*8+j];
    for(int n=0;n<2;++n) for(int j=0;j<8;++j) b[n][j] = sB[(lane>>4)*8+j][wc*32+n*16+(lane&15)];
    for(int m=0;m<2;++m) for(int n=0;n<2;++n) acc[m][n] = __builtin_amdgcn_mfma_f32_16x16x32_bf16(a[m], b[n], acc[m][n],0,0,0);
    __syncthreads();
  }
  for(int m=0;m<2;++m) for(int n=0;n<2;++n) for(int j=0;j<4;++j)
    C[(long)(bm+wr*32+m*16+(lane>>4)*4+j)*ldc + bn+wc*32+n*16+(lane&15)] = f2bf(acc[m][n][j]);
}

__device__ __forceinline__ float phi(float t){ float r = t>0.f ? rb(t+1.f) : rb(__expf(t)); return rb(r+1e-6f); }

__global__ __launch_bounds__(512) void kv_kernel(const u16* qkv, float* kv, float* z){
  int h = blockIdx.x; int t = threadIdx.x;
  if(t<400){ int d=t/20, e=t%20; float s=0.f;
    for(int i=0;i<2048;++i){ s += phi(bf2f(qkv[(long)i*1920+640+h*20+d]))*bf2f(qkv[(long)i*1920+1280+h*20+e]); }
    kv[h*400+t]=rb(s);
  } else if(t<420){ int d=t-400; float s=0.f;
    for(int i=0;i<2048;++i) s += phi(bf2f(qkv[(long)i*1920+640+h*20+d]));
    z[h*20+d]=rb(s);
  }
}
__global__ void o_kernel(const u16* qkv, const float* kv, const float* z, u16* o){
  int idx = blockIdx.x*blockDim.x+threadIdx.x; if(idx>=2048*640) return;
  int s = idx/640, c = idx%640, h=c/20, e=c%20;
  float num=0.f, den=0.f;
  for(int d=0;d<20;++d){ float pq = phi(bf2f(qkv[(long)s*1920+h*20+d])); num += pq*kv[h*400+d*20+e]; den += pq*z[h*20+d]; }
  num = rb(num); den = rb(rb(den)+1e-6f);
  o[idx] = f2bf(num/den);
}
__global__ void h_kernel(const u16* g, const u16* u, u16* hh){
  int idx = blockIdx.x*blockDim.x+threadIdx.x; if(idx>=2048*1536) return;
  float gg = bf2f(g[idx]); float sg = rb(1.f/(1.f+__expf(-gg)));
  hh[idx] = f2bf(sg*bf2f(u[idx]));
}
__global__ void final_kernel(const u16* x, const u16* gw, const u16* a, const u16* b, u16* out){
  int s = blockIdx.x; int t = threadIdx.x;
  float sum=0.f;
  for(int i=t;i<640;i+=64) sum += rb(bf2f(x[s*640+i])*bf2f(gw[i]));
  for(int o=32;o>0;o>>=1) sum += __shfl_xor(sum,o);
  float g = rb(1.f/(1.f+__expf(-rb(sum))));
  float omg = rb(1.f-g);
  for(int i=t;i<640;i+=64) out[s*640+i] = f2bf(rb(g*bf2f(a[s*640+i])) + rb(omg*bf2f(b[s*640+i])));
}

extern "C" void kernel_launch(void* const* d_in, const int* in_sizes, int n_in,
                              void* d_out, int out_size, void* d_ws, size_t ws_size,
                              hipStream_t stream) {
  const u16* x = (const u16*)d_in[0];
  const u16* wqkv = (const u16*)d_in[1];
  const u16* wo = (const u16*)d_in[2];
  const u16* wg = (const u16*)d_in[4];
  const u16* wu = (const u16*)d_in[5];
  const u16* wd = (const u16*)d_in[6];
  const u16* gw = (const u16*)d_in[7];
  char* ws = (char*)d_ws;
  u16* qkv = (u16*)ws; ws += 2048*1920*2;
  u16* g = (u16*)ws; ws += 2048*1536*2;
  u16* u = (u16*)ws; ws += 2048*1536*2;
  u16* hh = (u16*)ws; ws += 2048*1536*2;
  u16* o = (u16*)ws; ws += 2048*640*2;
  u16* a = (u16*)ws; ws += 2048*640*2;
  u16* b = (u16*)ws; ws += 2048*640*2;
  float* kv = (float*)ws; ws += 32*400*4;
  float* z = (float*)ws; ws += 32*20*4;
  gemm_simple<<<dim3(1920/64,32),256,0,stream>>>(x,640,wqkv,1920,qkv,1920,640);
  gemm_simple<<<dim3(1536/64,32),256,0,stream>>>(x,640,wg,1536,g,1536,640);
  gemm_simple<<<dim3(1536/64,32),256,0,stream>>>(x,640,wu,1536,u,1536,640);
  kv_kernel<<<32,512,0,stream>>>(qkv,kv,z);
  o_kernel<<<2048*640/256,256,0,stream>>>(qkv,kv,z,o);
  h_kernel<<<2048*1536/256,256,0,stream>>>(g,u,hh);
  gemm_simple<<<dim3(640/64,32),256,0,stream>>>(o,640,wo,640,a,640,640);
  gemm_simple<<<dim3(640/64,32),256,0,stream>>>(hh,1536,wd,640,b,640,1536);
  final_kernel<<<2048,64,0,stream>>>(x,gw,a,b,(u16*)d_out);
}
```

```cpp
#include <hip/hip_runtime.h>
#include <stdint.h>
#define LAS __attribute__((address_space(3)))
typedef unsigned short u16;
typedef short bf16x8 __attribute__((ext_vector_type(8)));
typedef short s16x4 __attribute__((ext_vector_type(4)));
typedef float f32x4 __attribute__((ext_vector_type(4)));
typedef float f32x2 __attribute__((ext_vector_type(2)));
typedef unsigned u32x4 __attribute__((ext_vector_type(4)));
typedef unsigned u32x2 __attribute__((ext_vector_type(2)));
typedef __bf16 bf16x2_t __attribute__((ext_vector_type(2)));
typedef __amdgpu_buffer_rsrc_t rsrc_t;
#pragma clang diagnostic ignored "-Wint-to-pointer-cast"

constexpr int SEQ = 2048, DM = 640, FF = 1536;
constexpr unsigned HF_OFF = 0,                 HF_SZ = 128u * 48 * 1024;
constexpr unsigned PF_OFF = HF_OFF + HF_SZ,    PF_SZ = 128u * 20 * 1024;
constexpr unsigned MF_OFF = PF_OFF + PF_SZ,    MF_SZ = 8u * 20 * 5 * 1024;
constexpr unsigned ZF_OFF = MF_OFF + MF_SZ,    ZF_SZ = 8u * 20 * 64;
constexpr unsigned KV_OFF = ZF_OFF + ZF_SZ,    KV_SZ = 8u * 640 * 96;
constexpr unsigned G_OFF = KV_OFF + KV_SZ,     G_SZ = 2048u * 4;
constexpr unsigned INIT_SZ = 256u * 40 * 1024;
static_assert(G_OFF + G_SZ <= INIT_SZ, "init region");
constexpr unsigned WD_OFF = INIT_SZ,           WD_SZ = 8u * 48 * 5 * 1024;
constexpr unsigned EP_OFF = WD_OFF + WD_SZ, FA_OFF = EP_OFF + 1024, FB_OFF = FA_OFF + 1024;
constexpr unsigned WS_USED = FB_OFF + 1024;
constexpr int LDS_BYTES = 163840;
constexpr int SC1 = 16;
constexpr unsigned NW = 0xFFFFFFFFu;
constexpr int SPIN_CAP = 1 << 15;

#define STAMP(i)
#define STAMP_FLUSH(w)
#define STAMP_ARG
#define STAMP_PASS

__device__ __forceinline__ rsrc_t mkrsrc(const void* p, unsigned bytes) { return __builtin_amdgcn_make_buffer_rsrc((void*)p, 0, bytes, 0x00020000); }
__device__ __forceinline__ unsigned pk2(float a, float b) { f32x2 v = {a, b}; bf16x2_t r = __builtin_convertvector(v, bf16x2_t); return __builtin_bit_cast(unsigned, r); }
__device__ __forceinline__ unsigned pk2(f32x2 v) { bf16x2_t r = __builtin_convertvector(v, bf16x2_t); return __builtin_bit_cast(unsigned, r); }
__device__ __forceinline__ f32x4 mfma(u32x4 a, u32x4 b, f32x4 c) { return __builtin_amdgcn_mfma_f32_16x16x32_bf16(__builtin_bit_cast(bf16x8, a), __builtin_bit_cast(bf16x8, b), c, 0, 0, 0); }
__device__ __forceinline__ u32x4 cat(u32x2 a, u32x2 b) { return u32x4{a.x, a.y, b.x, b.y}; }
#define BAR() do { asm volatile("" ::: "memory"); __builtin_amdgcn_s_barrier(); asm volatile("" ::: "memory"); } while (0)
#define SCHED0() __builtin_amdgcn_sched_barrier(0)
#define WAIT_LDS() asm volatile("s_waitcnt lgkmcnt(0)" ::: "memory")
template <int N> __device__ __forceinline__ void waitv() { asm volatile("s_waitcnt vmcnt(%0)" ::"i"(N) : "memory"); }
constexpr float LOG2E = 1.4426950408889634f, EPS = 1e-6f;
__device__ __forceinline__ unsigned phi2(float t0, float t1) {
  const f32x2 a = f32x2{t0, t1} * LOG2E + (-EPS * LOG2E);
  const f32x2 e = {__builtin_amdgcn_fmed3f(__builtin_amdgcn_exp2f(a.x), 0.f, 1.f), __builtin_amdgcn_fmed3f(__builtin_amdgcn_exp2f(a.y), 0.f, 1.f)};
  const int ie = __builtin_bit_cast(int, EPS);
  const f32x2 m = {__builtin_bit_cast(float, __builtin_elementwise_max(__builtin_bit_cast(int, t0), ie)), __builtin_bit_cast(float, __builtin_elementwise_max(__builtin_bit_cast(int, t1), ie))};
  return pk2(m + e);
}
__device__ __forceinline__ unsigned glu2(float g0, float g1, float u0, float u1) {
  const f32x2 a = f32x2{g0, g1} * (-LOG2E);
  const f32x2 d = f32x2{__builtin_amdgcn_exp2f(a.x), __builtin_amdgcn_exp2f(a.y)} + 1.f;
  return pk2(f32x2{u0, u1} * f32x2{__builtin_amdgcn_rcpf(d.x), __builtin_amdgcn_rcpf(d.y)});
}
__device__ __forceinline__ u32x4 ld16(rsrc_t r, unsigned voff, unsigned soff) { return __builtin_amdgcn_raw_buffer_load_b128(r, voff, soff, SC1); }
__device__ __forceinline__ unsigned ld4(rsrc_t r, unsigned voff, unsigned soff) { return __builtin_amdgcn_raw_buffer_load_b32(r, voff, soff, SC1); }
__device__ __forceinline__ void st16(u32x4 v, rsrc_t r, unsigned voff, unsigned soff) { __builtin_amdgcn_raw_buffer_store_b128(v, r, voff, soff, SC1); }
__device__ __forceinline__ void st8(u32x2 v, rsrc_t r, unsigned voff, unsigned soff) { __builtin_amdgcn_raw_buffer_store_b64(v, r, voff, soff, SC1); }
__device__ __forceinline__ void st4(unsigned v, rsrc_t r, unsigned voff, unsigned soff) { __builtin_amdgcn_raw_buffer_store_b32(v, r, voff, soff, SC1); }
__device__ __forceinline__ unsigned umax3(unsigned a, unsigned b, unsigned c) { return __builtin_elementwise_max(__builtin_elementwise_max(a, b), c); }
__device__ __forceinline__ unsigned acc_nw(unsigned m, u32x4 v) { return umax3(umax3(m, v.x, v.y), v.z, v.w); }
__device__ __forceinline__ bool any_nw(unsigned m) { return __builtin_amdgcn_ballot_w64(m == NW) != 0; }

constexpr int STG = 53248;
constexpr unsigned OOB = 0x1000000u;
__device__ __forceinline__ u32x4 mksrd(const void* p, unsigned bytes) { const uint64_t a = (uint64_t)p; return u32x4{(unsigned)a, (unsigned)(a >> 32) & 0xffffu, bytes, 0x00020000u}; }
template <int LOFF, int GOFF> __device__ __forceinline__ void dma(u32x4 srd, unsigned voff, unsigned lbase, unsigned gbase) {
  unsigned t;
  asm volatile("s_add_u32 m0, %2, %5\n\ts_add_u32 %0, %3, %6\n\tbuffer_load_dwordx4 %1, %4, %0 offen lds" : "=&s"(t) : "v"(voff), "s"(lbase), "s"(gbase), "s"(srd), "i"(LOFF), "i"(GOFF) : "scc");
}
__device__ __forceinline__ u32x4 l128(unsigned a) { return *(LAS u32x4*)(a); }
__device__ __forceinline__ u32x2 ltr(unsigned a) { s16x4 v = __builtin_amdgcn_ds_read_tr16_b64_v4i16((LAS s16x4*)(a)); return __builtin_bit_cast(u32x2, v); }

struct Ctx {
  rsrc_t rws; u32x4 sws; unsigned tag, vo; int bid, w, rb, cb; bool dead;
};
__device__ __forceinline__ bool flags_ok(u32x4 v, unsigned tag) { return __builtin_amdgcn_ballot_w64(v.x != tag || v.y != tag || v.z != tag || v.w != tag) == 0; }
__device__ __forceinline__ void flags_wait(Ctx& c, u32x4 v, unsigned off) {
  if (__builtin_expect(!flags_ok(v, c.tag), 0)) {
    int spin = 0;
    while (!c.dead) {
      __builtin_amdgcn_s_sleep(2);
      asm volatile("buffer_load_dwordx4 %0, %1, %2, %3 offen sc1\n\ts_waitcnt vmcnt(0)" : "+v"(v) : "v"(c.vo), "s"(c.sws), "s"(off) : "memory");
      if (flags_ok(v, c.tag)) break;
      if (++spin > SPIN_CAP) c.dead = true;
    }
  }
  asm volatile("" ::: "memory");
}

template <int GEO> struct Plan {
  u32x4 sa, sb; unsigned va, vb[GEO ? 3 : 1], la, lb[GEO ? 3 : 1]; bool lo;
  template <int I> __device__ __forceinline__ void issue(unsigned slot, unsigned ga, unsigned gb) const {
    if constexpr (GEO == 0) {
      if constexpr (I < 2) dma<I * 8192, I * 64 * 1280>(sa, va, slot + la, ga);
      else if constexpr (I < 6) dma<(I - 2) * 4096, (I - 2) * 16 * 3072>(sb, vb[0], slot + lb[0], gb);
      else dma<16384, 128 * 1280>(sa, va, slot + la, ga);
    } else {
      if constexpr (I < 4) dma<I * 8192, I * 64 * 1280>(sa, va, slot + la, ga);
      else dma<0, 0>(sb, vb[I - 4], slot + lb[I - 4], gb);
    }
  }
  __device__ __forceinline__ void issue_all(unsigned slot, unsigned ga, unsigned gb) const {
    issue<0>(slot, ga, gb); issue<1>(slot, ga, gb); issue<2>(slot, ga, gb); issue<3>(slot, ga, gb); issue<4>(slot, ga, gb); issue<5>(slot, ga, gb);
    if (lo) issue<6>(slot, ga, gb);
  }
};

template <int GEO> struct KState {
  u32x4 a[GEO ? 4 : 5], b[GEO ? 5 : 4];
  unsigned a0, bx, by, fill, ga, gb; int slot;
};

template <int GEO, bool SWAP, int MODE, class Extra>
__device__ __forceinline__ void stage(const Plan<GEO>& pl, KState<GEO>& k, f32x4 (&acc)[GEO ? 4 : 5][GEO ? 5 : 4], Extra&& extra) {
  constexpr int MT = GEO ? 4 : 5, NT = GEO ? 5 : 4, R = 2, RB = GEO ? 5 : 4;
  auto rdB = [&](int hh, int n) -> u32x4 {
    const unsigned base = (n & 1) ? k.by : k.bx;
    const unsigned imm = GEO ? (n & ~1) * 32 + hh * 10240 : (n >> 1) * 16384 + hh * 8192;
    constexpr unsigned T1 = GEO ? 1280 : 1024;
    return cat(ltr(base + imm), ltr(base + imm + T1));
  };
#pragma unroll
  for (int hh = 0; hh < 2; ++hh) {
#pragma unroll
    for (int n = 0; n < NT; ++n) {
      const int gi = hh * NT + n;
      const bool last = MODE == 3 && hh == 1;
      if (hh == 1 && n == NT - R && MODE != 3) {
        const int d = k.slot == 2 ? -2 * STG : STG;
        k.slot = k.slot == 2 ? 0 : k.slot + 1;
        k.bx += d; k.by += d; k.a0 += d;
        SCHED0();
        if (MODE == 0) asm volatile("s_waitcnt vmcnt(8) lgkmcnt(0)" ::: "memory");
        else if (MODE == 1) asm volatile("s_waitcnt vmcnt(8) lgkmcnt(0)" ::: "memory");
        else WAIT_LDS();
        BAR();
      }
      {
        const int pos = (gi + R) % (2 * NT);
        const int lv = 3 - pos * 4 / (2 * NT), lvp = 3 - ((pos + 2 * NT - 1) % (2 * NT)) * 4 / (2 * NT);
        if (lv != lvp) { if (lv == 3) __builtin_amdgcn_s_setprio(3); else if (lv == 2) __builtin_amdgcn_s_setprio(2); else if (lv == 1) __builtin_amdgcn_s_setprio(1); else __builtin_amdgcn_s_setprio(0); SCHED0(); }
      }
      const bool rb_ = !(last && n + R >= NT), ra_ = n == NT - 1 && !last;
      if (rb_) k.b[(gi + R) % RB] = n + R < NT ? rdB(hh, n + R) : rdB(hh ^ 1, n + R - NT);
#pragma unroll
      for (int m = 0; m < MT; ++m) {
        acc[m][n] = SWAP ? mfma(k.a[m], k.b[gi % RB], acc[m][n]) : mfma(k.b[gi % RB], k.a[m], acc[m][n]);
        if (ra_) k.a[m] = l128((hh == 0 ? (k.a0 ^ 64u) : k.a0) + m * 2048);
      }
      if (ra_) {
        __builtin_amdgcn_sched_group_barrier(0x100, 2, 0);
#pragma unroll
        for (int i = 0; i < MT; ++i) { __builtin_amdgcn_sched_group_barrier(0x8, 1, 0); __builtin_amdgcn_sched_group_barrier(0x100, 1, 0); }
      } else if (rb_) {
        __builtin_amdgcn_sched_group_barrier(0x8, 1, 0); __builtin_amdgcn_sched_group_barrier(0x100, 1, 0);
        __builtin_amdgcn_sched_group_barrier(0x8, 1, 0); __builtin_amdgcn_sched_group_barrier(0x100, 1, 0);
        __builtin_amdgcn_sched_group_barrier(0x8, MT - 2, 0);
      }
      SCHED0();
      if (MODE <= 1) {
        if (gi == 0) { if (pl.lo) { pl.template issue<0>(k.fill, k.ga, k.gb); pl.template issue<1>(k.fill, k.ga, k.gb); } }
        if (gi == 1) { if (!pl.lo) { pl.template issue<0>(k.fill, k.ga, k.gb); pl.template issue<1>(k.fill, k.ga, k.gb); } }
        if (gi == 2) { if (pl.lo) { pl.template issue<2>(k.fill, k.ga, k.gb); pl.template issue<3>(k.fill, k.ga, k.gb); } }
        if (gi == 3) { if (!pl.lo) { pl.template issue<2>(k.fill, k.ga, k.gb); pl.template issue<3>(k.fill, k.ga, k.gb); } }
        if (gi == 4) { if (pl.lo) { pl.template issue<4>(k.fill, k.ga, k.gb); pl.template issue<5>(k.fill, k.ga, k.gb); pl.template issue<6>(k.fill, k.ga, k.gb); } }
        if (gi == 5) { if (!pl.lo) { pl.template issue<4>(k.fill, k.ga, k.gb); pl.template issue<5>(k.fill, k.ga, k.gb); } }
      }
      extra(gi);
      SCHED0();
    }
  }
  k.fill = k.fill == 2 * STG ? 0 : k.fill + STG;
  k.ga += 128; k.gb += GEO ? 64u * 3840u : 64u * 3072u;
}

__device__ __forceinline__ u32x4 ldwd(Ctx& c, int i) { asm volatile("" : "+v"(c.vo)); return ld16(c.rws, c.vo + (i & 3) * 1024, WD_OFF + (c.cb * 48 + 6 * c.w) * 5120 + (i >> 2) * 4096); }

template <int GEO, bool SWAP, class X9>
__device__ __forceinline__ void kloop(Ctx& c, const Plan<GEO>& pl, unsigned ga0, unsigned gb0, unsigned a0, unsigned bx, unsigned by,
                                      f32x4 (&acc)[GEO ? 4 : 5][GEO ? 5 : 4], u32x4 (&wf)[30], X9&& x9 STAMP_ARG) {
  constexpr int MT = GEO ? 4 : 5;
  KState<GEO> k;
  k.a0 = a0; k.bx = bx; k.by = by; k.slot = 0; k.fill = 2 * STG; k.ga = ga0 + 256; k.gb = gb0 + 2 * (GEO ? 64u * 3840u : 64u * 3072u);
  BAR();
  STAMP(1);
#pragma unroll
  for (int m = 0; m < MT; ++m) k.a[m] = l128(a0 + m * 2048);
  k.b[0] = cat(ltr(bx), ltr(bx + (GEO ? 1280 : 1024)));
  k.b[1] = cat(ltr(by), ltr(by + (GEO ? 1280 : 1024)));
  const u32x4 nw4 = {NW, NW, NW, NW};
  unsigned io = (c.bid * 40 + c.w * 5 + 1) * 1024;
#pragma unroll 1
  for (int S = 0; S < 7; ++S) {
    stage<GEO, SWAP, 0>(pl, k, acc, [&](int gi) {
      if (gi == 5) st16(nw4, c.rws, c.vo, S < 4 ? io : OOB);
      if (gi == (GEO ? 9 : 7) && c.w == 0) {
        if (S == 1) { if (c.vo == 0) st4(c.tag, c.rws, c.bid * 4, FA_OFF); }
        if (S == 5) { if (c.vo == 0) st4(c.tag, c.rws, c.bid * 4, FB_OFF); }
      }
    });
    io += 1024;
  }
  u32x4 fl;
  stage<GEO, SWAP, 1>(pl, k, acc, [&](int gi) { if (gi == 5) fl = ld16(c.rws, c.vo, FA_OFF); });
  waitv<0>();
  flags_wait(c, fl, FA_OFF);
  stage<GEO, SWAP, 2>(pl, k, acc, [&](int gi) { if (gi < 8) wf[gi] = ldwd(c, gi); });
  stage<GEO, SWAP, 3>(pl, k, acc, [&](int gi) { if (gi == 0) { fl = ld16(c.rws, c.vo, FB_OFF); x9(); } if (gi < 8) wf[8 + gi] = ldwd(c, 8 + gi); });
  __builtin_amdgcn_s_setprio(0);
  flags_wait(c, fl, FB_OFF);
  STAMP(2);
}

__device__ __forceinline__ unsigned scale2(unsigned v, float s) { return pk2(f32x2{__builtin_bit_cast(float, v << 16), __builtin_bit_cast(float, v & 0xffff0000u)} * s); }

__device__ __forceinline__ void wait_words(Ctx& c, unsigned voff, unsigned soff) {
  int spin = 0;
  while (!c.dead) { const unsigned v = ld4(c.rws, voff, soff); if (!any_nw(v)) break; __builtin_amdgcn_s_sleep(2); if (++spin > SPIN_CAP) c.dead = true; }
  asm volatile("" ::: "memory");
}
template <int OFF> __device__ __forceinline__ void reld(const Ctx& c, u32x4& v, unsigned voff, unsigned soff) {
  asm volatile("buffer_load_dwordx4 %0, %1, %2, %3 offen offset:%4 sc1\n\ts_waitcnt vmcnt(0)" : "+v"(v) : "v"(voff), "s"(c.sws), "s"(soff), "i"(OFF) : "memory");
}
__device__ __forceinline__ void reld1(const Ctx& c, unsigned& v, unsigned voff, unsigned soff) {
  asm volatile("buffer_load_dword %0, %1, %2, %3 offen sc1\n\ts_waitcnt vmcnt(0)" : "+v"(v) : "v"(voff), "s"(c.sws), "s"(soff) : "memory");
}
#define RETRY(cond, reload) do { if (__builtin_expect((cond), 0)) { int spin_ = 0; while (!c.dead) { __builtin_amdgcn_s_sleep(2); reload; if (!(cond)) break; if (++spin_ > SPIN_CAP) c.dead = true; } } } while (0)
#define PIN(x) asm volatile("" : "+v"(x))
__device__ __forceinline__ int fresh_lane() { int l = __builtin_amdgcn_mbcnt_hi(~0u, __builtin_amdgcn_mbcnt_lo(~0u, 0u)); asm volatile("" : "+v"(l)); return l; }
__device__ __forceinline__ unsigned opq(unsigned v) { asm volatile("" : "+v"(v)); return v; }

constexpr int RG = 21504, PQX = 4 * RG, WO_LDS = 131072;

__device__ __forceinline__ void phase2(Ctx& c, u32x4 (&wf)[30], u16* __restrict__ out STAMP_ARG) {
  const int w = c.w, rb = c.rb, cb = c.cb;
  int lane = fresh_lane();
  unsigned vo = lane * 16;
  STAMP(4);
  const int t5 = (4 * rb) % 5, m0 = t5 >= 2 ? 5 - t5 : 0;
  f32x4 acc[4][5];
  u32x4 hf[3][3];
  const unsigned hbase = HF_OFF + (4 * rb * 48 + 6 * w) * 1024;
  unsigned rdy = 0;
  auto need = [&](int j) {
    int spin = 0;
    while (!((rdy >> j) & 1) && !c.dead) {
      const unsigned v = ld4(c.rws, (((m0 + fresh_lane()) & 3) * 48 + 5) * 1024 + 1020, hbase);
      rdy = (unsigned)__builtin_amdgcn_ballot_w64(v != NW);
      if (!((rdy >> j) & 1)) { __builtin_amdgcn_s_sleep(2); if (++spin > SPIN_CAP) c.dead = true; }
    }
    asm volatile("" ::: "memory");
  };
  auto ldh = [&](int k) {
    const unsigned so = hbase + ((m0 + (k >> 1)) & 3) * 49152 + (k & 1) * 3072;
    vo = opq(vo);
#pragma unroll
    for (int b = 0; b < 3; ++b) hf[k % 3][b] = ld16(c.rws, vo + b * 1024, so);
  };
  need(0); ldh(0); ldh(1);
  STAMP(5);
#pragma unroll
  for (int k = 0; k < 8; ++k) {
    if (k + 2 < 8) { if (!(k & 1)) need((k + 2) >> 1); ldh(k + 2); }
    SCHED0();
    RETRY(any_nw(acc_nw(acc_nw(acc_nw(0u, hf[k % 3][0]), hf[k % 3][1]), hf[k % 3][2])),
          { const unsigned so = hbase + ((m0 + (k >> 1)) & 3) * 49152 + (k & 1) * 3072; reld<0>(c, hf[k % 3][0], vo, so); reld<1024>(c, hf[k % 3][1], vo, so); reld<2048>(c, hf[k % 3][2], vo, so); });
#pragma unroll
    for (int b = 0; b < 3; ++b)
#pragma unroll
      for (int n = 0; n < 5; ++n) acc[k >> 1][n] = mfma(wf[(3 * (k & 1) + b) * 5 + n], hf[k % 3][b], (b == 0 && !(k & 1)) ? f32x4{0.f, 0.f, 0.f, 0.f} : acc[k >> 1][n]);
    SCHED0();
  }
  STAMP(6);
  lane = fresh_lane(); vo = lane * 16;
  const int g = lane >> 4, l15 = lane & 15;
  unsigned gv[4];
#pragma unroll
  for (int j = 0; j < 4; ++j) gv[j] = ld4(c.rws, l15 * 4, G_OFF + (64 * rb + 16 * ((m0 + j) & 3)) * 4);
  const int p = w & 3, mh = w >> 2;
  u32x4 pq[5][2], zf[5], mf[25];
  const unsigned pb0 = PF_OFF + ((4 * rb + ((m0 + 2 * mh) & 3)) * 20 + 5 * p) * 1024, pb1 = PF_OFF + ((4 * rb + ((m0 + 2 * mh + 1) & 3)) * 20 + 5 * p) * 1024;
  auto ldpq = [&]() {
    vo = opq(vo);
#pragma unroll
    for (int j = 0; j < 5; ++j) { pq[j][0] = ld16(c.rws, vo + (j & 3) * 1024, pb0 + (j >> 2) * 4096); pq[j][1] = ld16(c.rws, vo + (j & 3) * 1024, pb1 + (j >> 2) * 4096); }
  };
  ldpq();
  RETRY(any_nw(umax3(umax3(gv[0], gv[1], gv[2]), gv[3], 0u)), { _Pragma("unroll") for (int j = 0; j < 4; ++j) reld1(c, gv[j], l15 * 4, G_OFF + (64 * rb + 16 * ((m0 + j) & 3)) * 4); });
#pragma unroll
  for (int j = 0; j < 4; ++j) {
    const float om = 1.f - __builtin_bit_cast(float, gv[j]);
#pragma unroll
    for (int n = 0; n < 5; ++n) { acc[j][n] *= om; PIN(acc[j][n]); }
  }
  if (w >= 4) {
#pragma unroll
    for (int j = 0; j < 4; ++j)
#pragma unroll
      for (int n = 0; n < 5; ++n) *(LAS f32x4*)((w - 4) * RG + (j * 5 + n) * 1024 + vo) = acc[j][n];
  }
  SCHED0();
  const unsigned zb = ZF_OFF + (cb * 20 + 5 * p) * 64;
  auto ldz = [&]() {
#pragma unroll
    for (int j = 0; j < 5; ++j) zf[j] = ld16(c.rws, opq(g * 16) + j * 64, zb);
  };
  {
    auto bad = [&]() { unsigned m = 0;
#pragma unroll
      for (int j = 0; j < 5; ++j) m = acc_nw(acc_nw(m, pq[j][0]), pq[j][1]);
      return any_nw(m); };
    RETRY(bad(), { _Pragma("unroll") for (int j = 0; j < 5; ++j) { reld<0>(c, pq[j][0], vo, pb0 + j * 1024); reld<0>(c, pq[j][1], vo, pb1 + j * 1024); } });
  }
  STAMP(7);
  ldz();
  {
    auto bad = [&]() { unsigned m = 0;
#pragma unroll
      for (int j = 0; j < 5; ++j) m = acc_nw(m, zf[j]);
      return any_nw(m); };
    RETRY(bad(), { _Pragma("unroll") for (int j = 0; j < 5; ++j) reld<0>(c, zf[j], g * 16, zb + j * 64); });
  }
  STAMP(8);
  const unsigned mb = MF_OFF + (cb * 20 + 5 * p) * 5120;
  auto ldm = [&](int lo, int hi) {
    vo = opq(vo);
#pragma unroll
    for (int i = lo; i < hi; ++i) mf[i] = ld16(c.rws, vo + (i & 3) * 1024, mb + (i >> 2) * 4096);
  };
  if (w < 4) ldm(0, 10);
  SCHED0();
  {
    constexpr int zq[9] = {0, 0, 0, 1, 1, 1, 1, 2, 2}, zj[9] = {0, 1, 2, 1, 2, 3, 4, 3, 4};
    f32x4 den[2][3];
#pragma unroll
    for (int i = 0; i < 9; ++i) {
      const int q = zq[i], j = zj[i];
      const int n = 4 * q + (l15 & 3);
      const int hrow = n < 10 ? (16 * n + 4 * (l15 >> 2)) / 20 : -1;
      const bool lo = (32 * j + 4 * g) / 20 == hrow, hi = (32 * j + 16 + 4 * g) / 20 == hrow;
      const u32x4 zm = {lo ? zf[j].x : 0u, lo ? zf[j].y : 0u, hi ? zf[j].z : 0u, hi ? zf[j].w : 0u};
      const bool first = i == 0 || zq[i - 1] != q;
#pragma unroll
      for (int m = 0; m < 2; ++m) den[m][q] = mfma(zm, pq[j][m], first ? f32x4{EPS, EPS, EPS, EPS} : den[m][q]);
    }
#pragma unroll
    for (int m = 0; m < 2; ++m) {
      const float gm = __builtin_bit_cast(float, mh ? gv[2 + m] : gv[m]);
#pragma unroll
      for (int j = 0; j < 5; ++j) {
        const float s0 = gm * __builtin_amdgcn_rcpf(den[m][(2 * j) >> 2][(2 * j) & 3]);
        const float s1 = gm * __builtin_amdgcn_rcpf(den[m][(2 * j + 1) >> 2][(2 * j + 1) & 3]);
        pq[j][m] = u32x4{scale2(pq[j][m].x, s0), scale2(pq[j][m].y, s0), scale2(pq[j][m].z, s1), scale2(pq[j][m].w, s1)};
        PIN(pq[j][m]);
      }
    }
  }
  if (w >= 4) {
#pragma unroll
    for (int j = 0; j < 5; ++j) { *(LAS u32x4*)(PQX + p * 10240 + j * 2048 + vo) = pq[j][0]; *(LAS u32x4*)(PQX + p * 10240 + j * 2048 + 1024 + vo) = pq[j][1]; }
  }
  SCHED0();
  if (w < 4) ldm(10, 20);
  WAIT_LDS();
  BAR();
  STAMP(9);
  if (w < 4) {
#pragma unroll
    for (int j = 0; j < 4; ++j) {
#pragma unroll
      for (int n = 0; n < 5; ++n) { acc[j][n] += *(LAS f32x4*)(w * RG + (j * 5 + n) * 1024 + vo); PIN(acc[j][n]); }
      SCHED0();
    }
    ldm(20, 25);
    SCHED0();
#pragma unroll
    for (int j = 0; j < 5; ++j) {
      const u32x4 o0 = l128(PQX + p * 10240 + j * 2048 + vo), o1 = l128(PQX + p * 10240 + j * 2048 + 1024 + vo);
      auto bad = [&]() { unsigned m = 0;
#pragma unroll
        for (int n = 0; n < 5; ++n) m = acc_nw(m, mf[5 * j + n]);
        return any_nw(m); };
      RETRY(bad(), { _Pragma("unroll") for (int n = 0; n < 5; ++n) reld<0>(c, mf[5 * j + n], vo, mb + (5 * j + n) * 1024); });
#pragma unroll
      for (int n = 0; n < 5; ++n) { acc[0][n] = mfma(mf[5 * j + n], pq[j][0], acc[0][n]); acc[1][n] = mfma(mf[5 * j + n], pq[j][1], acc[1][n]); }
#pragma unroll
      for (int n = 0; n < 5; ++n) { acc[2][n] = mfma(mf[5 * j + n], o0, acc[2][n]); acc[3][n] = mfma(mf[5 * j + n], o1, acc[3][n]); }
      SCHED0();
    }
#pragma unroll
    for (int j = 0; j < 4; ++j)
#pragma unroll
      for (int n = 0; n < 5; ++n) *(LAS f32x4*)(w * RG + (16 * j + l15) * 336 + (16 * n + 4 * g) * 4) = acc[j][n];
    WAIT_LDS();
  }
  STAMP(10);
  BAR();
  const int tid = w * 64 + fresh_lane();
#pragma unroll
  for (int it = 0; it < 2; ++it) {
    const int pc = tid + 512 * it;
    if (pc < 640) {
      const int tok = pc / 10, c8 = pc % 10;
      f32x4 s0 = {0.f, 0.f, 0.f, 0.f}, s1 = s0;
#pragma unroll
      for (int k = 0; k < 4; ++k) { s0 += *(LAS f32x4*)(k * RG + tok * 336 + c8 * 32); s1 += *(LAS f32x4*)(k * RG + tok * 336 + c8 * 32 + 16); }
      const u32x4 o = {pk2(s0[0], s0[1]), pk2(s0[2], s0[3]), pk2(s1[0], s1[1]), pk2(s1[2], s1[3])};
      const int row = 64 * rb + 16 * ((m0 + (tok >> 4)) & 3) + (tok & 15);
      *(u32x4*)(out + (size_t)row * 640 + 80 * cb + 8 * c8) = o;
    }
  }
  STAMP(11);
  STAMP_FLUSH(w);
}

struct In { const u16 *x, *wqkv, *wo, *wg, *wu, *wd, *gw; u16* out; };

__device__ __forceinline__ void role_gate(Ctx& c, const In& in, int r0 STAMP_ARG) {
  const int w = c.w, bid = c.bid, lane = fresh_lane(), tid = w * 64 + lane, g = lane >> 4, l15 = lane & 15;
  c.vo = lane * 16;
  const u32x4 nw4 = {NW, NW, NW, NW};
  u32x4 wf[30];
  const u16 *x = in.x, *gw = in.gw;
    asm volatile("" : "+s"(c.tag));
#pragma unroll
    for (int i = 0; i < 5; ++i) st16(nw4, c.rws, lane * 16, (bid * 40 + w * 5 + i) * 1024);
    u32x4 gv[5];
#pragma unroll
    for (int i = 0; i < 5; ++i) gv[i] = *(const u32x4*)((const char*)gw + (l15 + 16 * i) * 16);
    const char* xp = (const char*)x + (size_t)(r0 * 512 + w * 64 + g) * 1280 + l15 * 16;
    float gres[4];
#pragma unroll
    for (int it4 = 0; it4 < 4; ++it4) {
      u32x4 v[4][5];
#pragma unroll
      for (int k = 0; k < 4; ++k)
#pragma unroll
        for (int i = 0; i < 5; ++i) v[k][i] = *(const u32x4*)(xp + (size_t)(it4 * 4 + k) * 4 * 1280 + i * 256);
      SCHED0();
      float s[4];
#pragma unroll
      for (int k = 0; k < 4; ++k) {
        s[k] = 0.f;
#pragma unroll
        for (int i = 0; i < 5; ++i)
#pragma unroll
          for (int j = 0; j < 4; ++j) { const unsigned a = v[k][i][j], b = gv[i][j]; s[k] = __builtin_amdgcn_fdot2_f32_bf16(__builtin_bit_cast(bf16x2_t, a), __builtin_bit_cast(bf16x2_t, b), s[k], false); }
      }
#pragma unroll
      for (int o = 1; o < 16; o <<= 1)
#pragma unroll
        for (int k = 0; k < 4; ++k) s[k] += __shfl_xor(s[k], o);
      const float sv = l15 == 0 ? s[0] : l15 == 1 ? s[1] : l15 == 2 ? s[2] : s[3];
      gres[it4] = __builtin_amdgcn_rcpf(1.f + __builtin_amdgcn_exp2f(-sv * LOG2E));
      if (it4 == 0) {
        waitv<0>(); BAR();
        if (tid == 0) { st4(c.tag, c.rws, bid * 4, FA_OFF); st4(c.tag, c.rws, bid * 4, FB_OFF); }
      }
      SCHED0();
    }
    STAMP(1);
    flags_wait(c, ld16(c.rws, lane * 16, FA_OFF), FA_OFF);
#pragma unroll
    for (int i = 0; i < 30; ++i) wf[i] = ldwd(c, i);
    flags_wait(c, ld16(c.rws, lane * 16, FB_OFF), FB_OFF);
    STAMP(2);
    if (l15 < 4) {
#pragma unroll
      for (int it4 = 0; it4 < 4; ++it4) st4(__builtin_bit_cast(unsigned, gres[it4]), c.rws, (l15 * 4 + g) * 4, G_OFF + (r0 * 512 + w * 64 + it4 * 16) * 4);
    }
    STAMP(3);
  phase2(c, wf, in.out STAMP_PASS);
}

template <int type> __device__ __forceinline__ void role_gemm(Ctx& c, const In& in, int r0, int cc STAMP_ARG) {
  const int w = c.w, bid = c.bid, lane = fresh_lane(), tid = w * 64 + lane, g = lane >> 4, l15 = lane & 15;
  c.vo = lane * 16;
  const u32x4 nw4 = {NW, NW, NW, NW};
  u32x4 wf[30];
  const u16 *x = in.x, *wqkv = in.wqkv, *wo = in.wo, *wg = in.wg, *wu = in.wu, *wd = in.wd;
  const int r8 = lane >> 3, c8 = lane & 7, q4 = l15 >> 2, p4 = l15 & 3;
  const unsigned va = (unsigned)(8 * w + r8) * 1280u + ((c8 ^ ((4 * (w & 1) + (r8 >> 1)) & 7)) * 16);
  const int nb = bid < 156 ? bid : bid - 4;
  const bool rt = w < 2 && nb < 240;
  const int ru = 2 * nb + w, rkb = ru / 10, rc64 = ru % 10;
  auto retile_issue = [&]() {
    if (rt) {
      const u32x4 sd = mksrd(wd, FF * DM * 2);
      const unsigned vd = (unsigned)r8 * 1280u + ((((c8 >> 1) ^ ((r8 >> 1) & 3)) << 1 | (c8 & 1)) * 16);
      const unsigned ld = 2 * STG + w * 4096, gd = rkb * 32u * 1280u + rc64 * 128u;
      dma<0, 0>(sd, vd, ld, gd); dma<1024, 8 * 1280>(sd, vd, ld, gd); dma<2048, 16 * 1280>(sd, vd, ld, gd); dma<3072, 24 * 1280>(sd, vd, ld, gd);
    }
  };
  auto retile = [&]() {
    st16(nw4, c.rws, lane * 16, (bid * 40 + w * 5) * 1024);
    asm volatile("" : "+s"(c.tag));
    if (rt) {
      waitv<8>();
      const int row = 4 * g + q4;
#pragma unroll
      for (int n = 0; n < 4; ++n) {
        const unsigned ad = 2 * STG + w * 4096 + row * 128 + ((n ^ ((row >> 1) & 3)) * 32) + p4 * 8;
        u32x4 fr = cat(ltr(ad), ltr(ad + 2048));
        const int nt = 4 * rc64 + n, cb = nt / 5, n5 = nt % 5;
        st16(fr, c.rws, lane * 16, WD_OFF + ((cb * 48 + rkb) * 5 + n5) * 1024);
      }
    } else waitv<7>();
  };
  auto bump = [&]() { if (bid == 0 && w == 0 && c.vo == 0) st4(c.tag, c.rws, 0, EP_OFF); };

  if constexpr (type == 0) {
    Plan<0> pl;
    pl.sa = mksrd(x, SEQ * DM * 2); pl.sb = mksrd(w >= 4 ? wu : wg, DM * FF * 2); pl.lo = w < 4;
    pl.va = va; pl.la = w * 1024;
    const int bt = w & 1, hj = (w >> 1) & 1, p0 = 2 * bt + hj;
    pl.vb[0] = (unsigned)(4 * p0 + g) * 3072u + (((l15 >> 1) ^ (g | (bt << 2))) * 32) + (l15 & 1) * 16;
    pl.lb[0] = 20480 + (w >> 2) * 16384 + p0 * 1024;
    const int tr0 = r0 < 12 ? 10 * r0 : 118;
    const unsigned ga0 = tr0 * 16u * 1280u, gb0 = cc * 256u;
    pl.issue_all(0, ga0, gb0); retile_issue(); pl.issue_all(STG, ga0 + 128, gb0 + 64u * 3072u);
    retile();
    const int wr = w >> 2, wc = w & 3;
    f32x4 acc[5][4];
#pragma unroll
    for (int m = 0; m < 5; ++m)
#pragma unroll
      for (int n = 0; n < 4; ++n) acc[m][n] = f32x4{0.f, 0.f, 0.f, 0.f};
    const unsigned a0 = (wr * 80 + l15) * 128 + ((g ^ (l15 >> 1)) * 16);
    const int f = q4 | ((g & 1) << 2);
    const unsigned bb = 20480 + (8 * g + q4) * 256 + p4 * 8;
    kloop<0, false>(c, pl, ga0, gb0, a0, bb + (((2 * wc) ^ f) * 32), bb + (((2 * wc + 1) ^ f) * 32), acc, wf, []() {} STAMP_PASS);
    bump();
    const int t0 = tr0 + wr * 5;
#pragma unroll
    for (int m = 0; m < 5; ++m) {
      const u32x4 o = {glu2(acc[m][0][0], acc[m][0][1], acc[m][2][0], acc[m][2][1]), glu2(acc[m][0][2], acc[m][0][3], acc[m][2][2], acc[m][2][3]),
                       glu2(acc[m][1][0], acc[m][1][1], acc[m][3][0], acc[m][3][1]), glu2(acc[m][1][2], acc[m][1][3], acc[m][3][2], acc[m][3][3])};
      st16(o, c.rws, c.vo, t0 + m >= 10 * r0 ? HF_OFF + ((t0 + m) * 48 + 4 * cc + wc) * 1024 : OOB);
#pragma unroll
      for (int i = 16 + 3 * m; i < 16 + 3 * m + 3; ++i) if (i < 30) wf[i] = ldwd(c, i);
      SCHED0();
    }
    BAR();
    STAMP(3);
  } else {
  Plan<1> pl;
  pl.sa = mksrd(x, SEQ * DM * 2); pl.sb = mksrd(wqkv, DM * 1920 * 2); pl.lo = w < 4;
  pl.va = va; pl.la = w * 1024;
#pragma unroll
  for (int i = 0; i < 3; ++i) {
    const unsigned p = i < 2 ? w + 8 * i : 16 + (w & 3);
    const unsigned o = 1024 * p + 16 * lane, r = o / 320, cb = o % 320;
    const unsigned sl = (cb >> 5) ^ ((r >> 3) & 1);
    const unsigned kvoff = sl < 3 ? sl * 32 : sl < 5 ? 1280 + (sl - 3) * 32 : sl < 7 ? (sl - 2) * 32 : 1280 + (sl - 5) * 32;
    pl.vb[i] = r * 3840u + (type == 2 ? kvoff : sl * 32) + (cb & 16);
    pl.lb[i] = 32768 + p * 1024;
  }
  const unsigned ga0 = r0 * 256u * 1280u, gb0 = type == 2 ? 1280u + cc * 160u : cc * 320u;
  pl.issue_all(0, ga0, gb0); retile_issue(); pl.issue_all(STG, ga0 + 128, gb0 + 64u * 3840u);
  retile();
  const int wr = w & 3, wc = w >> 2;
  const unsigned a0 = (wr * 64 + l15) * 128 + ((g ^ (l15 >> 1)) * 16);
  const unsigned bb = 32768 + (8 * g + q4) * 320 + p4 * 8;
  const unsigned bx = bb + (((5 * wc) ^ (g & 1)) * 32), by = bb + (((5 * wc + 1) ^ (g & 1)) * 32);
  if constexpr (type == 1) {
    f32x4 acc[4][5];
#pragma unroll
    for (int m = 0; m < 4; ++m)
#pragma unroll
      for (int n = 0; n < 5; ++n) acc[m][n] = f32x4{EPS, EPS, EPS, EPS};
    kloop<1, false>(c, pl, ga0, gb0, a0, bx, by, acc, wf, []() {} STAMP_PASS);
    const int t0 = r0 * 16 + wr * 4;
#pragma unroll
    for (int m = 0; m < 4; ++m) {
      u32x2 h[5];
#pragma unroll
      for (int n = 0; n < 5; ++n) h[n] = u32x2{phi2(acc[m][n][0], acc[m][n][1]), phi2(acc[m][n][2], acc[m][n][3])};
      const unsigned so = PF_OFF + ((t0 + m) * 20 + 5 * cc) * 1024;
      const u32x4 f0 = wc ? cat(h[1], h[2]) : cat(h[0], h[1]), f1 = wc ? cat(h[3], h[4]) : cat(h[2], h[3]);
      st16(f0, c.rws, c.vo, so + wc * 3072);
      st16(f1, c.rws, c.vo, so + wc * 3072 + 1024);
      st8(wc ? h[0] : h[4], c.rws, c.vo, so + 2048 + wc * 8);
#pragma unroll
      for (int i = 16 + 4 * m; i < 16 + 4 * m + 4; ++i) if (i < 30) wf[i] = ldwd(c, i);
      SCHED0();
    }
    BAR();
    STAMP(3);
  } else {
    f32x4 acc[4][5];
    const float e2 = wc ? 0.f : EPS;
#pragma unroll
    for (int m = 0; m < 4; ++m) {
      acc[m][0] = acc[m][1] = f32x4{EPS, EPS, EPS, EPS}; acc[m][2] = f32x4{e2, e2, e2, e2}; acc[m][3] = acc[m][4] = f32x4{0.f, 0.f, 0.f, 0.f};
    }
    const u32x4 so_ = mksrd(wo, DM * DM * 2);
    kloop<1, true>(c, pl, ga0, gb0, a0, bx, by, acc, wf, [&]() {
#pragma unroll
      for (int i = 0; i < 2; ++i) {
        const unsigned o = (w + 8 * i) * 1024 + opq(c.vo);
        if (w + 8 * i < 15) dma<0, 0>(so_, __builtin_elementwise_min(cc * 80u + o / 160, 639u) * 1280 + o % 160, WO_LDS + (w + 8 * i) * 1024, r0 * 160u);
      }
    } STAMP_PASS);
    const int lane = fresh_lane(), tid = w * 64 + lane, g = lane >> 4, l15 = lane & 15;
    auto fphi = [&](int kb, int n) { return u32x4{phi2(acc[2 * kb][n][0], acc[2 * kb][n][1]), phi2(acc[2 * kb][n][2], acc[2 * kb][n][3]),
                                                   phi2(acc[2 * kb + 1][n][0], acc[2 * kb + 1][n][1]), phi2(acc[2 * kb + 1][n][2], acc[2 * kb + 1][n][3])}; };
    auto fid = [&](int kb, int n) { return u32x4{pk2(acc[2 * kb][n][0], acc[2 * kb][n][1]), pk2(acc[2 * kb][n][2], acc[2 * kb][n][3]),
                                                  pk2(acc[2 * kb + 1][n][0], acc[2 * kb + 1][n][1]), pk2(acc[2 * kb + 1][n][2], acc[2 * kb + 1][n][3])}; };
    const u32x4 ones = {0x3F803F80u, 0x3F803F80u, 0x3F803F80u, 0x3F803F80u};
    const f32x4 z4 = {0.f, 0.f, 0.f, 0.f};
    const unsigned REG = STG, reg = REG + wr * 7680, xch = STG + 30720 + wr * 4096 + lane * 16;
    u32x4 f0[5], f1[5];
    f0[3] = fid(0, 3); f1[3] = fid(1, 3); f0[4] = fid(0, 4); f1[4] = fid(1, 4);
    if (wc == 1) {
      f0[2] = fid(0, 2); f1[2] = fid(1, 2);
      *(LAS u32x4*)(xch) = f0[2]; *(LAS u32x4*)(xch + 1024) = f1[2]; *(LAS u32x4*)(xch + 2048) = f0[3]; *(LAS u32x4*)(xch + 3072) = f1[3];
    }
    if (wc == 0) { f0[2] = fphi(0, 2); f1[2] = fphi(1, 2); }
    f0[0] = fphi(0, 0); f1[0] = fphi(1, 0); f0[1] = fphi(0, 1); f1[1] = fphi(1, 1);
    asm volatile("s_waitcnt vmcnt(8) lgkmcnt(0)" ::: "memory");
    BAR();
    auto prod = [&](int i, int j, u32x4 k0, u32x4 k1, u32x4 v0, u32x4 v1) {
      const int d = 16 * i + l15, er = 16 * j + 4 * g - 20 * (d / 20);
      f32x4 t = mfma(v1, k1, mfma(v0, k0, z4));
      if (er >= 0 && er < 20) *(LAS f32x4*)(reg + d * 96 + er * 4) = t;
    };
    auto zsum = [&](int i, u32x4 k0, u32x4 k1) { f32x4 t = mfma(ones, k1, mfma(ones, k0, z4)); if (g == 0) *(LAS f32x4*)(reg + (16 * i + l15) * 96 + 80) = t; };
    if (wc == 0) {
      const u32x4 v20 = l128(xch), v21 = l128(xch + 1024), v30 = l128(xch + 2048), v31 = l128(xch + 3072);
      prod(0, 0, f0[0], f1[0], f0[3], f1[3]); prod(0, 1, f0[0], f1[0], f0[4], f1[4]);
      prod(1, 0, f0[1], f1[1], f0[3], f1[3]); prod(1, 1, f0[1], f1[1], f0[4], f1[4]); prod(2, 1, f0[2], f1[2], f0[4], f1[4]);
      zsum(0, f0[0], f1[0]); zsum(1, f0[1], f1[1]); zsum(2, f0[2], f1[2]);
      prod(1, 2, f0[1], f1[1], v20, v21); prod(2, 2, f0[2], f1[2], v20, v21); prod(2, 3, f0[2], f1[2], v30, v31);
    }
    if (wc == 1) {
      prod(3, 2, f0[0], f1[0], f0[2], f1[2]); prod(3, 3, f0[0], f1[0], f0[3], f1[3]); prod(3, 4, f0[0], f1[0], f0[4], f1[4]);
      prod(4, 3, f0[1], f1[1], f0[3], f1[3]); prod(4, 4, f0[1], f1[1], f0[4], f1[4]);
      zsum(3, f0[0], f1[0]); zsum(4, f0[1], f1[1]);
    }
    WAIT_LDS();
    BAR();
    if (tid < 480) {
      const unsigned p = REG + tid * 16;
      f32x4 s = *(LAS f32x4*)(p) + *(LAS f32x4*)(7680 + p) + *(LAS f32x4*)(15360 + p) + *(LAS f32x4*)(23040 + p);
      st16(__builtin_bit_cast(u32x4, s), c.rws, tid * 16, KV_OFF + (r0 * 640 + cc * 80) * 96);
    }
#pragma unroll
    for (int i = 16; i < 30; ++i) wf[i] = ldwd(c, i);
    WAIT_LDS();
    BAR();
    STAMP(3);
    if (w < 5) {
      const int lane = fresh_lane(), g = lane >> 4, l15 = lane & 15, q4 = l15 >> 2, p4 = l15 & 3;
      const int dl = 16 * w + l15, hd = dl / 20, hlo = (16 * w) / 20, hhi = (16 * w + 15) / 20;
      const unsigned kb = KV_OFF + (cc * 80 + 16 * w) * 96;
      wait_words(c, (lane & 7) * 61440 + 15 * 96 + 92, kb);
      u32x4 p0[8], p1[8];
      const unsigned pv = l15 * 96 + (g < 3 ? g : 2) * 32;
      auto ldp = [&]() {
#pragma unroll
        for (int r = 0; r < 8; ++r) { p0[r] = ld16(c.rws, pv, kb + r * 61440); p1[r] = ld16(c.rws, pv + 16, kb + r * 61440); }
      };
      ldp();
      auto bad = [&]() { unsigned m = 0;
#pragma unroll
        for (int r = 0; r < 8; ++r) m = acc_nw(acc_nw(m, p0[r]), p1[r]);
        return any_nw(m); };
      RETRY(bad(), { _Pragma("unroll") for (int r = 0; r < 8; ++r) { reld<0>(c, p0[r], pv, kb + r * 61440); reld<16>(c, p1[r], pv, kb + r * 61440); } });
      f32x4 s0 = __builtin_bit_cast(f32x4, p0[0]), s1 = __builtin_bit_cast(f32x4, p1[0]);
#pragma unroll
      for (int r = 1; r < 8; ++r) { s0 += __builtin_bit_cast(f32x4, p0[r]); s1 += __builtin_bit_cast(f32x4, p1[r]); }
      SCHED0();
      u32x4 bw[2][5];
#pragma unroll
      for (int hh = 0; hh < 2; ++hh)
#pragma unroll
        for (int n = 0; n < 5; ++n) {
          const unsigned ad = WO_LDS + (20 * (hh ? hhi : hlo) + 8 * g + q4) * 160 + n * 32 + p4 * 8;
          bw[hh][n] = cat(ltr(ad), ltr(ad + 640));
        }
      const int T = 5 * cc + w, blk = __builtin_amdgcn_readfirstlane(r0 * 20 + (T >> 1)), half = T & 1;
      const float zn = __shfl_down(s1[0], 1);
      if (g == 2 && !(l15 & 1)) st4(pk2(s1[0], zn), c.rws, (l15 >> 2) * 16 + (l15 & 2) * 2, ZF_OFF + blk * 64 + half * 8);
      u32x4 af = {pk2(s0[0], s0[1]), pk2(s0[2], s0[3]), pk2(s1[0], s1[1]), pk2(s1[2], s1[3])};
      if (g >= 2) { af.z = 0; af.w = 0; }
      if (g == 3) { af.x = 0; af.y = 0; }
      const u32x4 zero = {0, 0, 0, 0};
      const u32x4 alo = hd == hlo ? af : zero, ahi = (hd == hhi && hhi != hlo) ? af : zero;
#pragma unroll
      for (int n = 0; n < 5; ++n) {
        const f32x4 t = mfma(ahi, bw[1][n], mfma(alo, bw[0][n], z4));
        st8(u32x2{pk2(t[0], t[1]), pk2(t[2], t[3])}, c.rws, lane * 16 + half * 8, MF_OFF + (blk * 5 + n) * 1024);
      }
    }
  }
  }
  phase2(c, wf, in.out STAMP_PASS);
}

__global__ __launch_bounds__(512, 2) void k_fused(const u16* __restrict__ x, const u16* __restrict__ wqkv, const u16* __restrict__ wo, const u16* __restrict__ wg,
                                                  const u16* __restrict__ wu, const u16* __restrict__ wd, const u16* __restrict__ gw, char* ws, u16* __restrict__ out
) {
  const int w = __builtin_amdgcn_readfirstlane(threadIdx.x >> 6);
  const int bid = blockIdx.x, xcd = bid & 7, idx = bid >> 3, xi = xcd >> 2, xj = xcd & 3;
  STAMP(0);
  const int i3 = (idx * 11) >> 5;
  const bool mid = idx == 18 || idx == 19;
  const int type = idx >= 28 ? 1 : idx >= 20 ? 2 : (idx == 19 && xi) ? 3 : 0;
  const int r0 = idx >= 28 ? 4 * xi + idx - 28 : idx >= 20 ? 4 * xi + ((idx - 20) >> 1) : mid ? (type == 3 ? xj : 6) : 7 * xi + i3;
  const int cc = idx >= 28 ? xj : idx >= 20 ? 2 * xj + (idx & 1) : mid ? 3 * xj + (xi ? 2 : idx - 18) : 3 * xj + idx - 3 * i3;
  Ctx c;
  c.rws = mkrsrc(ws, WS_USED); c.sws = mksrd(ws, WS_USED); c.bid = bid; c.w = w; c.vo = 0; c.rb = 4 * xcd + (idx >> 3); c.cb = idx & 7; c.dead = false;
  c.tag = *(const __attribute__((address_space(4))) unsigned*)(uintptr_t)(ws + EP_OFF) * 0x9E3779B1u + 0x7F4A7C15u;
  const In in = {x, wqkv, wo, wg, wu, wd, gw, out};
  if (type == 0) role_gemm<0>(c, in, r0, cc STAMP_PASS);
  else if (type == 2) role_gemm<2>(c, in, r0, cc STAMP_PASS);
  else if (type == 1) role_gemm<1>(c, in, r0, cc STAMP_PASS);
  else role_gate(c, in, r0 STAMP_PASS);
}

extern "C" void kernel_launch(void* const* d_in, const int* in_sizes, int n_in, void* d_out, int out_size, void* d_ws, size_t ws_size, hipStream_t stream) {
  (void)hipFuncSetAttribute((const void*)k_fused, hipFuncAttributeMaxDynamicSharedMemorySize, LDS_BYTES);
  k_fused<<<256, 512, LDS_BYTES, stream>>>((const u16*)d_in[0], (const u16*)d_in[1], (const u16*)d_in[2], (const u16*)d_in[4], (const u16*)d_in[5], (const u16*)d_in[6],
                                           (const u16*)d_in[7], (char*)d_ws, (u16*)d_out
  );
}
```

```cpp
#include <hip/hip_runtime.h>
#include <stdint.h>
#define LAS __attribute__((address_space(3)))
typedef unsigned short u16;
typedef short bf16x8 __attribute__((ext_vector_type(8)));
typedef short s16x4 __attribute__((ext_vector_type(4)));
typedef float f32x4 __attribute__((ext_vector_type(4)));
typedef float f32x2 __attribute__((ext_vector_type(2)));
typedef unsigned u32x4 __attribute__((ext_vector_type(4)));
typedef unsigned u32x2 __attribute__((ext_vector_type(2)));
typedef unsigned u32x16 __attribute__((ext_vector_type(16)));
typedef __bf16 bf16x2_t __attribute__((ext_vector_type(2)));
typedef __amdgpu_buffer_rsrc_t rsrc_t;
#pragma clang diagnostic ignored "-Wint-to-pointer-cast"

constexpr int SEQ = 2048, DM = 640, FF = 1536;
constexpr unsigned MF_OFF = 0,                 MF_SZ = 8u * 20 * 5 * 1024;
constexpr unsigned ZF_OFF = MF_OFF + MF_SZ,    ZF_SZ = 8u * 20 * 64;
constexpr unsigned KVS = 13 * 1024 + 320;
constexpr unsigned KV_OFF = ZF_OFF + ZF_SZ,    KV_SZ = 64u * KVS;
constexpr unsigned G_OFF = KV_OFF + KV_SZ,     G_SZ = 2048u * 8;
constexpr unsigned INIT_W = 896, INIT_SZ = 2048u * INIT_W;
static_assert(G_OFF + G_SZ <= INIT_SZ, "init region");
constexpr unsigned HF_OFF = INIT_SZ,           HF_SZ = 128u * 48 * 1024;
constexpr unsigned PF_OFF = HF_OFF + HF_SZ,    PF_SZ = 128u * 20 * 1024;
constexpr unsigned WD_OFF = PF_OFF + PF_SZ,    WD_SZ = 8u * 48 * 5 * 1024;
constexpr unsigned EP_OFF = WD_OFF + WD_SZ, FA_OFF = EP_OFF + 1024, FH_OFF = FA_OFF + 1024;
constexpr unsigned WS_USED = FH_OFF + 256 * 64;
#define CODE_BYTES 40960u
constexpr int LDS_BYTES = 163840;
constexpr int SC1 = 16;
constexpr unsigned NW = 0xFFFFFFFFu;
constexpr int SPIN_CAP = 1 << 15;

#define STAMP(i)
#define STAMPV(i)
#define STAMP_FLUSH(w)
#define STAMP_ARG
#define STAMP_PASS

__device__ __forceinline__ rsrc_t mkrsrc(const void* p, unsigned bytes) { return __builtin_amdgcn_make_buffer_rsrc((void*)p, 0, bytes, 0x00020000); }
__device__ __forceinline__ unsigned pk2(float a, float b) { f32x2 v = {a, b}; bf16x2_t r = __builtin_convertvector(v, bf16x2_t); return __builtin_bit_cast(unsigned, r); }
__device__ __forceinline__ unsigned pk2(f32x2 v) { bf16x2_t r = __builtin_convertvector(v, bf16x2_t); return __builtin_bit_cast(unsigned, r); }
__device__ __forceinline__ f32x4 mfma(u32x4 a, u32x4 b, f32x4 c) { return __builtin_amdgcn_mfma_f32_16x16x32_bf16(__builtin_bit_cast(bf16x8, a), __builtin_bit_cast(bf16x8, b), c, 0, 0, 0); }
__device__ __forceinline__ u32x4 cat(u32x2 a, u32x2 b) { return u32x4{a.x, a.y, b.x, b.y}; }
#define BAR() do { asm volatile("" ::: "memory"); __builtin_amdgcn_s_barrier(); asm volatile("" ::: "memory"); } while (0)
#define SCHED0() __builtin_amdgcn_sched_barrier(0)
#define WAIT_LDS() asm volatile("s_waitcnt lgkmcnt(0)" ::: "memory")
template <int N> __device__ __forceinline__ void waitv() { asm volatile("s_waitcnt vmcnt(%0)" ::"i"(N) : "memory"); }
constexpr float LOG2E = 1.4426950408889634f, EPS = 1e-6f;
__device__ __forceinline__ unsigned phi2(float t0, float t1) {
  const f32x2 a = f32x2{t0, t1} * LOG2E + (-EPS * LOG2E);
  const f32x2 e = {__builtin_amdgcn_fmed3f(__builtin_amdgcn_exp2f(a.x), 0.f, 1.f), __builtin_amdgcn_fmed3f(__builtin_amdgcn_exp2f(a.y), 0.f, 1.f)};
  const int ie = __builtin_bit_cast(int, EPS);
  const f32x2 m = {__builtin_bit_cast(float, __builtin_elementwise_max(__builtin_bit_cast(int, t0), ie)), __builtin_bit_cast(float, __builtin_elementwise_max(__builtin_bit_cast(int, t1), ie))};
  return pk2(m + e);
}
__device__ __forceinline__ u32x4 phi8(f32x4& t0, f32x4& t1) {
  const f32x2 t[4] = {{t0[0], t0[1]}, {t0[2], t0[3]}, {t1[0], t1[1]}, {t1[2], t1[3]}};
  f32x2 e[4], m[4];
  const int ie = __builtin_bit_cast(int, EPS);
#pragma unroll
  for (int i = 0; i < 4; ++i) e[i] = t[i] * LOG2E + (-EPS * LOG2E);
#pragma unroll
  for (int i = 0; i < 4; ++i) { const float x = t[i].x, y = t[i].y; m[i] = f32x2{__builtin_bit_cast(float, __builtin_elementwise_max(__builtin_bit_cast(int, x), ie)), __builtin_bit_cast(float, __builtin_elementwise_max(__builtin_bit_cast(int, y), ie))}; }
#pragma unroll
  for (int i = 0; i < 4; ++i) e[i] = f32x2{__builtin_amdgcn_fmed3f(__builtin_amdgcn_exp2f(e[i].x), 0.f, 1.f), __builtin_amdgcn_fmed3f(__builtin_amdgcn_exp2f(e[i].y), 0.f, 1.f)};
  __builtin_amdgcn_sched_group_barrier(0x2, 4, 0); __builtin_amdgcn_sched_group_barrier(0x400, 2, 0); __builtin_amdgcn_sched_group_barrier(0x2, 4, 0); __builtin_amdgcn_sched_group_barrier(0x400, 3, 0);
  __builtin_amdgcn_sched_group_barrier(0x2, 4, 0); __builtin_amdgcn_sched_group_barrier(0x400, 3, 0); __builtin_amdgcn_sched_group_barrier(0x2, 8, 0);
#pragma unroll
  for (int i = 0; i < 4; ++i) m[i] += e[i];
  t0 = f32x4{m[0].x, m[0].y, m[1].x, m[1].y}; t1 = f32x4{m[2].x, m[2].y, m[3].x, m[3].y};
  return u32x4{pk2(m[0]), pk2(m[1]), pk2(m[2]), pk2(m[3])};
}
__device__ __forceinline__ unsigned glu2(float g0, float g1, float u0, float u1, float oi) {
  const f32x2 a = f32x2{g0, g1} * (-LOG2E);
  const f32x2 d = f32x2{__builtin_amdgcn_exp2f(a.x), __builtin_amdgcn_exp2f(a.y)} * oi + oi;
  return pk2(f32x2{u0, u1} * f32x2{__builtin_amdgcn_rcpf(d.x), __builtin_amdgcn_rcpf(d.y)});
}
__device__ __forceinline__ u32x4 ld16(rsrc_t r, unsigned voff, unsigned soff) { return __builtin_amdgcn_raw_buffer_load_b128(r, voff, soff, SC1); }
__device__ __forceinline__ unsigned ld4(rsrc_t r, unsigned voff, unsigned soff) { return __builtin_amdgcn_raw_buffer_load_b32(r, voff, soff, SC1); }
template <int AUX = SC1> __device__ __forceinline__ void st16(u32x4 v, rsrc_t r, unsigned voff, unsigned soff) { __builtin_amdgcn_raw_buffer_store_b128(v, r, voff, soff, AUX); asm volatile("s_nop 1" ::"v"(v)); }
__device__ __forceinline__ void st8(u32x2 v, rsrc_t r, unsigned voff, unsigned soff) { __builtin_amdgcn_raw_buffer_store_b64(v, r, voff, soff, SC1); }
__device__ __forceinline__ void st4(unsigned v, rsrc_t r, unsigned voff, unsigned soff) { __builtin_amdgcn_raw_buffer_store_b32(v, r, voff, soff, SC1); }
__device__ __forceinline__ unsigned umax3(unsigned a, unsigned b, unsigned c) { return __builtin_elementwise_max(__builtin_elementwise_max(a, b), c); }
__device__ __forceinline__ unsigned acc_nw(unsigned m, u32x4 v) { return umax3(umax3(m, v.x, v.y), v.z, v.w); }
__device__ __forceinline__ bool any_nw(unsigned m) { return __builtin_amdgcn_ballot_w64(m == NW) != 0; }

#define HPM 4
#define HPT 4
#define LPM 2
#define LPT 2
#define Z_DELAY 30
#define MW 2
#define PSL 8
#define PAUX 16
#define F_DELAY 16
#define MPRE 20
#define QPRE 2
#define TOUCH
#define RT_FIRST
constexpr int TW = 1;
constexpr int STG = 53248;
constexpr int SPARE = 3 * STG, RT_LDS = 2 * STG + 28672;
constexpr unsigned OOB = 0x1000000u;
__device__ __forceinline__ u32x4 mksrd(const void* p, unsigned bytes) { const uint64_t a = (uint64_t)p; return u32x4{(unsigned)a, (unsigned)(a >> 32) & 0xffffu, bytes, 0x00020000u}; }
template <int LOFF, int GOFF, int DEV = 0> __device__ __forceinline__ void dma(u32x4 srd, unsigned voff, unsigned lbase, unsigned gbase) {
  unsigned t;
  if constexpr (DEV) asm volatile("s_add_u32 m0, %2, %5\n\ts_add_u32 %0, %3, %6\n\tbuffer_load_dwordx4 %1, %4, %0 offen sc1 lds" : "=&s"(t) : "v"(voff), "s"(lbase), "s"(gbase), "s"(srd), "i"(LOFF), "i"(GOFF) : "scc", "memory");
  else
  asm volatile("s_add_u32 m0, %2, %5\n\ts_add_u32 %0, %3, %6\n\tbuffer_load_dwordx4 %1, %4, %0 offen lds" : "=&s"(t) : "v"(voff), "s"(lbase), "s"(gbase), "s"(srd), "i"(LOFF), "i"(GOFF) : "scc");
}
__device__ __forceinline__ u32x4 l128(unsigned a) { return *(LAS u32x4*)(a); }
__device__ __forceinline__ u32x2 ltr(unsigned a) { s16x4 v = __builtin_amdgcn_ds_read_tr16_b64_v4i16((LAS s16x4*)(a)); return __builtin_bit_cast(u32x2, v); }

__device__ __forceinline__ int fresh_lane() { unsigned z; asm volatile("s_mov_b32 %0, 0" : "=s"(z)); const int l = __builtin_amdgcn_mbcnt_hi(~0u, __builtin_amdgcn_mbcnt_lo(~0u, z)); __builtin_assume(l >= 0 && l < 64); return l; }
#define PIN(x) asm volatile("" : "+v"(x))
struct Ctx {
  rsrc_t rws; u32x4 sws; unsigned tag, vo; int bid, w, tb, nt, cb;
  unsigned rdy; bool dead, rot;
};
__device__ __forceinline__ bool flags_ok(u32x4 v, unsigned tag) { return __builtin_amdgcn_ballot_w64(v.x != tag || v.y != tag || v.z != tag || v.w != tag) == 0; }
__device__ __forceinline__ void flags_wait(Ctx& c, u32x4 v, unsigned off) {
  if (__builtin_expect(!flags_ok(v, c.tag), 0)) {
    int spin = 0;
#pragma nounroll
    while (!c.dead) {
      __builtin_amdgcn_s_sleep(2);
      asm volatile("buffer_load_dwordx4 %0, %1, %2, %3 offen sc1\n\ts_waitcnt vmcnt(0)" : "+v"(v) : "v"(c.vo), "s"(c.sws), "s"(off) : "memory");
      if (flags_ok(v, c.tag)) break;
      if (++spin > SPIN_CAP) c.dead = true;
    }
  }
  asm volatile("" ::: "memory");
}

struct Plan {
  u32x4 sa, sb; unsigned va, vb[4], la, lb[4]; bool a2, a3, b2, b3, lo, rt;
  __device__ __forceinline__ void issue(unsigned slot, unsigned ga, unsigned gb) const {
    dma<0, 0>(sa, va, slot + la, ga); dma<8192, 64 * 1280>(sa, va, slot + la, ga);
    if (a2) dma<16384, 128 * 1280>(sa, va, slot + la, ga);
    if (a3) dma<24576, 192 * 1280>(sa, va, slot + la, ga);
    dma<0, 0>(sb, vb[0], slot + lb[0], gb); dma<0, 0>(sb, vb[1], slot + lb[1], gb);
    if (b2) dma<0, 0>(sb, vb[2], slot + lb[2], gb);
    if (b3) dma<0, 0>(sb, vb[3], slot + lb[3], gb);
  }
  template <int GEO> __device__ __forceinline__ void issue_lo(unsigned slot, unsigned ga, unsigned gb) const {
    dma<0, 0>(sa, va, slot + la, ga); dma<8192, 64 * 1280>(sa, va, slot + la, ga); dma<16384, 128 * 1280>(sa, va, slot + la, ga);
    if (GEO) dma<24576, 192 * 1280>(sa, va, slot + la, ga);
    dma<0, 0>(sb, vb[0], slot + lb[0], gb); dma<0, 0>(sb, vb[1], slot + lb[1], gb); dma<0, 0>(sb, vb[2], slot + lb[2], gb);
    if (!GEO) dma<0, 0>(sb, vb[3], slot + lb[3], gb);
  }
  template <int GEO> __device__ __forceinline__ void issue_hi(unsigned slot, unsigned ga, unsigned gb) const {
    dma<0, 0>(sa, va, slot + la, ga); dma<8192, 64 * 1280>(sa, va, slot + la, ga);
    if (GEO) { dma<16384, 128 * 1280>(sa, va, slot + la, ga); dma<24576, 192 * 1280>(sa, va, slot + la, ga); }
    dma<0, 0>(sb, vb[0], slot + lb[0], gb); dma<0, 0>(sb, vb[1], slot + lb[1], gb);
    if (!GEO) { dma<0, 0>(sb, vb[2], slot + lb[2], gb); dma<0, 0>(sb, vb[3], slot + lb[3], gb); }
  }
};

__device__ __forceinline__ unsigned plan_va(int w, int lane) { const int r8 = lane >> 3, c8 = lane & 7; return (unsigned)(8 * w + r8) * 1280u + ((c8 ^ ((4 * (w & 1) + (r8 >> 1)) & 7)) * 16); }
__device__ __forceinline__ void plan_vt(unsigned (&vb)[4], int w, int lane, bool isQ) {
  const unsigned long long lut = isQ ? 0ull | 1ull << 6 | 2ull << 12 | 3ull << 18 | 4ull << 24 | 5ull << 30 | 6ull << 36 | 7ull << 42 | 8ull << 48 | 9ull << 54
                                     : 0ull | 1ull << 6 | 2ull << 12 | 40ull << 18 | 41ull << 24 | 3ull << 30 | 4ull << 36 | 42ull << 42 | 43ull << 48 | 44ull << 54;
#pragma unroll
  for (int i = 0; i < 3; ++i) {
    const unsigned p = i < 2 ? w + 8 * i : 16 + (w & 3);
    const unsigned u = (64 * p + lane) & 2047u, r = (u * 3277u) >> 16, cu = u - 20 * r, sl = (cu >> 1) ^ ((r >> 3) & 1);
    vb[i] = r * 3840u + ((unsigned)(lut >> (6 * (sl & 15u))) & 63u) * 32 + (cu & 1) * 16;
  }
  vb[3] = 0;
}

template <int GEO> struct KState {
  u32x4 a[2][GEO ? 4 : 5], b[GEO ? 5 : 4];
  unsigned a0, bx, by, fill, ga, gb; int slot;
};

template <int GEO, bool SWAP, int MODE, int NW8, class Extra>
__device__ __forceinline__ void stage(const Plan& pl, KState<GEO>& k, f32x4 (&acc)[GEO ? 4 : 5][GEO ? 5 : 4], bool more, Extra&& extra) {
  constexpr int MT = GEO ? 4 : 5, NT = GEO ? 5 : 4, R = 2, RB = GEO ? 5 : 4;
  auto rdB = [&](int hh, int n) -> u32x4 {
    const unsigned base = (n & 1) ? k.by : k.bx;
    const unsigned imm = GEO ? (n & ~1) * 32 + hh * 10240 : (n >> 1) * 16384 + hh * 8192;
    constexpr unsigned T1 = GEO ? 1280 : 1024;
    return cat(ltr(base + imm), ltr(base + imm + T1));
  };
#pragma unroll
  for (int hh = 0; hh < 2; ++hh) {
#pragma unroll
    for (int n = 0; n < NT; ++n) {
      const int gi = hh * NT + n;
      const bool last = MODE == 3 && hh == 1;
      if (hh == 1 && n == NT - R && MODE != 3) {
        const int d = k.slot == 2 ? -2 * STG : STG;
        k.slot = k.slot == 2 ? 0 : k.slot + 1;
        k.bx += d; k.by += d; k.a0 += d;
        k.fill = k.fill == 2 * STG ? 0 : k.fill + STG;
        k.ga += 128; k.gb += GEO ? 64u * 3840u : 64u * 3072u;
        SCHED0();

        if (MODE == 0) asm volatile("s_waitcnt vmcnt(6) lgkmcnt(0)" ::: "memory");
        else asm volatile("s_waitcnt vmcnt(%0) lgkmcnt(0)" ::"i"(NW8) : "memory");
        BAR();
      }
      {
        const int pos = (gi + R) % (2 * NT);
        const int lv = 3 - pos * 4 / (2 * NT), lvp = 3 - ((pos + 2 * NT - 1) % (2 * NT)) * 4 / (2 * NT);
        if (lv != lvp) { if (lv == 3) __builtin_amdgcn_s_setprio(3); else if (lv == 2) __builtin_amdgcn_s_setprio(2); else if (lv == 1) __builtin_amdgcn_s_setprio(1); else __builtin_amdgcn_s_setprio(0); SCHED0(); }
      }
      int nds = 0;
      if (!(last && n + R >= NT)) { k.b[(gi + R) % RB] = n + R < NT ? rdB(hh, n + R) : rdB(hh ^ 1, n + R - NT); nds = 2; }
      if (!last) {
        const unsigned an = hh == 0 ? (k.a0 ^ 64u) : k.a0;
        if (n == NT - 2) { k.a[hh ^ 1][0] = l128(an); k.a[hh ^ 1][1] = l128(an + 2048); nds += 2; }
        if (n == NT - 1) {
#pragma unroll
          for (int m = 2; m < MT; ++m) k.a[hh ^ 1][m] = l128(an + m * 2048);
          nds += MT - 2;
        }
      }
#pragma unroll
      for (int m = 0; m < MT; ++m) acc[m][n] = SWAP ? mfma(k.a[hh][m], k.b[gi % RB], acc[m][n]) : mfma(k.b[gi % RB], k.a[hh][m], acc[m][n]);
#pragma unroll
      for (int i = 0; i < MT; ++i) { __builtin_amdgcn_sched_group_barrier(0x8, 1, 0); if (i < nds) __builtin_amdgcn_sched_group_barrier(0x100, 1, 0); }
      SCHED0();
      if (MODE == 0) {
        constexpr int LP = GEO ? LPT : LPM, HP = GEO ? HPT : HPM;
        const int pos = (gi + R) % (2 * NT);
        if (pos == LP) { if (pl.lo && (pos >= R || more)) pl.template issue_lo<GEO>(k.fill, k.ga, k.gb); }
        if (pos == HP) { if (!pl.lo && (pos >= R || more)) pl.template issue_hi<GEO>(k.fill, k.ga, k.gb); }
      }
      extra(gi);
      SCHED0();
    }
  }
}

__device__ __forceinline__ u32x4 ldwd(Ctx& c, int i) { asm volatile("" : "+v"(c.vo)); const int k = i < 15 ? i : i - 15;
  return ld16(c.rws, c.vo + (k & 3) * 1024, WD_OFF + (c.cb * 48 + 6 * c.w) * 5120 + ((i < 15) == c.rot ? 15360 : 0) + (k >> 2) * 4096); }

template <int GEO, bool SWAP, int WPRE, class X8, class X9>
__device__ __forceinline__ void kloop(Ctx& c, const Plan& pl, unsigned ga0, unsigned gb0, unsigned a0, unsigned bx, unsigned by,
                                      f32x4 (&acc)[GEO ? 4 : 5][GEO ? 5 : 4], u32x4 (&wf)[30], const u32x4 (&rf)[5], float gs, X8&& x8, X9&& x9 STAMP_ARG) {
  constexpr int MT = GEO ? 4 : 5;
  KState<GEO> k;
  k.a0 = a0; k.bx = bx; k.by = by; k.slot = 0; k.fill = 2 * STG; k.ga = ga0 + 256; k.gb = gb0 + 2 * (GEO ? 64u * 3840u : 64u * 3072u);
#pragma unroll
  for (int m = 0; m < MT; ++m)
#pragma unroll
    for (int n = 0; n < (GEO ? 5 : 4); ++n) asm volatile("" : "+v"(acc[m][n]));
  waitv<7 + TW>();
  BAR();
  STAMP(1);
#pragma unroll
  for (int m = 0; m < MT; ++m) k.a[0][m] = l128(a0 + m * 2048);
  k.b[0] = cat(ltr(bx), ltr(bx + (GEO ? 1280 : 1024)));
  k.b[1] = cat(ltr(by), ltr(by + (GEO ? 1280 : 1024)));
  if ((GEO ? LPT : LPM) < 2) { if (pl.lo) pl.template issue_lo<GEO>(k.fill, k.ga, k.gb); }
  if ((GEO ? HPT : HPM) < 2) { if (!pl.lo) pl.template issue_hi<GEO>(k.fill, k.ga, k.gb); }
#pragma unroll 1
  for (int S = 0; S < 8; ++S) {
    stage<GEO, SWAP, 0, 0>(pl, k, acc, S < 7, [&](int gi) {
      if (gi == (GEO ? 9 : 7)) {
        if (__builtin_expect((0x67 >> S) & 1, 0)) {
          if (S == 0) st16(u32x4{NW, NW, NW, NW}, c.rws, c.vo < INIT_W ? c.vo : OOB, (c.bid * 8 + c.w) * INIT_W);
          if (GEO == 0 && S < 2) {
            if (pl.rt) {
              const unsigned u = (c.bid - 128) * 3 + c.w - 4, ro = WD_OFF + ((u & 7) * 48 + (u >> 3)) * 5120;
              if (S == 0) { st16(rf[0], c.rws, c.vo, ro); st16(rf[1], c.rws, c.vo, ro + 1024); }
              else { st16(rf[2], c.rws, c.vo, ro + 2048); st16(rf[3], c.rws, c.vo, ro + 3072); st16(rf[4], c.rws, c.vo, ro + 4096); }
            }
          }
          if (S == 2) STAMP(19);
          if (S == 6) STAMP(20);
          if (S == 2) st4(c.tag, c.rws, c.vo ? OOB : c.bid * 4, c.w ? OOB : FA_OFF);
          if (S == 5) { if (c.w == 7) asm volatile("s_mov_b32 m0, %3\n\ts_nop 0\n\tbuffer_load_dwordx4 %0, %1, %2 offen sc1 lds" ::"v"(c.vo), "s"(c.sws), "s"(FA_OFF), "s"(SPARE + 2048) : "memory"); }
          if (S == 6) {
            flags_wait(c, l128(SPARE + 2048 + c.vo), FA_OFF);
            if (GEO == 0) st4(__builtin_bit_cast(unsigned, gs), c.rws, (c.vo & 224) ? OOB : ((c.vo & 16) << 9) + ((c.vo >> 8) << 2), (c.bid < 224 && c.w < 4) ? G_OFF + ((c.bid - 96) * 16 + c.w * 4) * 4 : OOB);
          }
        }
      }
    });
  }
  auto wl = [&](int base, int gi) {
    if (GEO) { if (base + gi < WPRE) wf[base + gi] = ldwd(c, base + gi); }
    else { wf[base + gi] = ldwd(c, base + gi); if (gi < 2) wf[base + 8 + gi] = ldwd(c, base + 8 + gi); }
  };
  stage<GEO, SWAP, 2, (WPRE ? 8 : 0)>(pl, k, acc, false, [&](int gi) { if (gi == 0) x8(); wl(0, gi); });
  STAMP(25);
  stage<GEO, SWAP, 3, 0>(pl, k, acc, false, [&](int gi) { if (gi == 0) x9(); wl(10, gi); });
  __builtin_amdgcn_s_setprio(0);
  STAMP(2);
}

__device__ __forceinline__ unsigned scale2(unsigned v, float s) { return pk2(f32x2{__builtin_bit_cast(float, v << 16), __builtin_bit_cast(float, v & 0xffff0000u)} * s); }

template <int SL = 1> __device__ __forceinline__ void wait_words(Ctx& c, unsigned voff, unsigned soff) {
  int spin = 0;
#pragma nounroll
  while (!c.dead) {
    unsigned v;
    asm volatile("buffer_load_dword %0, %1, %2, %3 offen sc1\n\ts_waitcnt vmcnt(0)" : "=&v"(v) : "v"(voff), "s"(c.sws), "s"(soff) : "memory");
    if (!any_nw(v)) break;
    __builtin_amdgcn_s_sleep(SL);
    if (++spin > SPIN_CAP) c.dead = true;
  }
}
template <int OFF> __device__ __forceinline__ void reld(const Ctx& c, u32x4& v, unsigned voff, unsigned soff) {
  asm volatile("s_nop 4\n\tbuffer_load_dwordx4 %0, %1, %2, %3 offen offset:%4 sc1\n\ts_waitcnt vmcnt(0)" : "+v"(v) : "v"(voff), "s"(c.sws), "s"(__builtin_amdgcn_readfirstlane(soff)), "i"(OFF) : "memory");
}
__device__ __forceinline__ void reld1(const Ctx& c, unsigned& v, unsigned voff, unsigned soff) {
  asm volatile("buffer_load_dword %0, %1, %2, %3 offen sc1\n\ts_waitcnt vmcnt(0)" : "+v"(v) : "v"(voff), "s"(c.sws), "s"(soff) : "memory");
}
#define RETRY(cond, reload) do { if (__builtin_expect((cond), 0)) { int spin_ = 0; _Pragma("nounroll") while (!c.dead) { __builtin_amdgcn_s_sleep(2); reload; if (!(cond)) break; if (++spin_ > SPIN_CAP) c.dead = true; } } } while (0)
__device__ __forceinline__ unsigned opq(unsigned v) { asm volatile("" : "+v"(v)); return v; }

constexpr int FX = STG, WO_LDS = 136192, DUMMY_ = 0;

constexpr unsigned long long zmask(int q, int d0) {
  unsigned long long m = 0;
  for (int l = 0; l < 64; ++l) { const int i = l & 15, n = 4 * q + (i & 3); if (n < 5 && (16 * n + 4 * (i >> 2)) / 20 == (d0 + 4 * (l >> 4)) / 20) m |= 1ull << l; }
  return m;
}
template <int Q, int D0> __device__ __forceinline__ bool zkeep() { constexpr unsigned long long m = zmask(Q, D0); return __builtin_amdgcn_inverse_ballot_w64(m); }
template <int CTRL, int BANKS> __device__ __forceinline__ unsigned dpp(unsigned old, unsigned v) { return (unsigned)__builtin_amdgcn_update_dpp((int)old, (int)v, CTRL, 0xf, BANKS, false); }
template <int CTRL> __device__ __forceinline__ float dadd(float v) { return v + __builtin_bit_cast(float, __builtin_amdgcn_update_dpp(0, __builtin_bit_cast(int, v), CTRL, 0xf, 0xf, true)); }

template <int P> __device__ __forceinline__ void setprio() { __builtin_amdgcn_s_setprio(P); }
#define RAMP(i) do { if ((i) == 0) setprio<3>(); else if ((i) == 1) setprio<2>(); else if ((i) == 2) setprio<1>(); else setprio<0>(); } while (0)

__device__ __forceinline__ unsigned flag_off(const Ctx& c, int lane) {
  const int t = c.tb + ((lane & 1) ? c.nt - 1 : 0), r13 = t >= 120 ? 12 : (t * 205) >> 11, fc = (6 * c.w + ((lane & 2) ? 5 : 0)) >> 2;
  return (lane & 4) ? (156 + (t >> 4) * 4 + (c.w & 3)) * 64 : (r13 * 12 + fc) * 64;
}
__device__ __forceinline__ u32x4 pk8(f32x4 a, f32x4 b) { return u32x4{pk2(a[0], a[1]), pk2(a[2], a[3]), pk2(b[0], b[1]), pk2(b[2], b[3])}; }
constexpr int DMP = 0, FIN = 0, T5A = 30720, T5M = 40960, PQX = 61440;
__device__ __forceinline__ void put5(unsigned d, unsigned vo, const f32x4 (&a)[5]) {
  *(LAS u32x4*)(d + vo) = pk8(a[0], a[1]);
  *(LAS u32x4*)(d + 1024 + vo) = pk8(a[2], a[3]);
  *(LAS u32x2*)(d + 2048 + (vo >> 1)) = u32x2{pk2(a[4][0], a[4][1]), pk2(a[4][2], a[4][3])};
}

template <int ROLE, int NT2> __device__ __forceinline__ void phase2(Ctx& c, u32x4 (&wf)[30], const u16* out STAMP_ARG) {
  const int w = c.w, cb = c.cb, p = w & 3;
  const int lane = fresh_lane(), g = lane >> 4, l15 = lane & 15;
  unsigned vo = lane * 16;
  STAMP(4);
  const int m0 = (cb + p) & 3;
  int tl[5];
#pragma unroll
  for (int m = 0; m < 4; ++m) tl[m] = c.tb + ((m0 + m) & 3);
  tl[4] = c.tb + c.nt - 1;
  const f32x4 z4 = {0.f, 0.f, 0.f, 0.f};
  f32x4 acc[5][5];
  u32x4 hf[5][6];
  const unsigned fo = flag_off(c, lane);
  auto need = [&](unsigned mask, bool slow) {
    int spin = 0;
#pragma nounroll
    while ((c.rdy & mask) != mask && !c.dead) {
      const unsigned v = ld4(c.rws, ((mask >> (lane & 7)) & 1) ? fo : OOB, FH_OFF);
      c.rdy |= (unsigned)__builtin_amdgcn_ballot_w64(v == c.tag);
      if ((c.rdy & mask) != mask) { if (slow) __builtin_amdgcn_s_sleep(PSL); else __builtin_amdgcn_s_sleep(2); if (++spin > SPIN_CAP) c.dead = true; }
    }
    asm volatile("" ::: "memory");
  };
  need(15u, false);
  STAMP(15);
  auto ldh = [&](int m) {
    vo = opq(vo);
#pragma unroll
    for (int b = 0; b < 6; ++b) hf[m][b] = ld16(c.rws, vo + (b % 3) * 1024, HF_OFF + (tl[m] * 48 + 6 * w) * 1024 + ((b < 3) == c.rot ? 3072 : 0));
    SCHED0();
  };
  auto mlp = [&](int m) {
#pragma unroll
    for (int b = 0; b < 6; ++b)
#pragma unroll
      for (int n = 0; n < 5; ++n) acc[m][n] = mfma(wf[b * 5 + n], hf[m][b], b == 0 ? z4 : acc[m][n]);
    SCHED0();
  };
  unsigned mb = MF_OFF + (cb * 20 + 5 * p) * 5120;
  asm volatile("" : "+s"(mb));
  unsigned mwatch;
  if (NT2 == 5) {
    setprio<3>(); ldh(4); setprio<2>(); ldh(0);
    STAMP(5);
    setprio<3>(); mlp(4); ldh(1);
    put5(T5M + w * 2560, vo, acc[4]);
    SCHED0();
  } else {
    setprio<3>(); ldh(0); setprio<2>(); ldh(1);
    STAMP(5);
  }
  setprio<3>(); mlp(0); ldh(2);
  STAMP(16);
  setprio<2>(); mlp(1); ldh(3);
  STAMP(17);
  setprio<1>(); mlp(2);
  mwatch = ld4(c.rws, w < 4 ? fo : (lane < 5 ? lane : 4) * 5120 + 5116, w < 4 ? FH_OFF : mb);
  SCHED0();
  STAMP(18);
  setprio<0>(); mlp(3);
  STAMP(6);
  vo = opq(vo);
  const unsigned one = (g == (l15 >> 2)) ? (0x3F80u << (16 * (l15 & 1))) : 0u;
  const u32x4 E0 = {(l15 & 2) ? 0u : one, (l15 & 2) ? one : 0u, 0u, 0u}, E1 = {0u, 0u, E0.x, E0.y};
  const rsrc_t ro = mkrsrc(out, SEQ * DM * 2);
  if (w < 4) {
    const unsigned l0 = PQX + p * 5120 * NT2;
    unsigned sq[5];
#pragma unroll
    for (int m = 0; m < 5; ++m) sq[m] = PF_OFF + (tl[m] * 20 + 5 * p) * 1024;
#define PQB(J) do { dma<J * NT2 * 1024, J * 1024, 1>(c.sws, vo, l0, sq[0]); dma<J * NT2 * 1024 + 1024, J * 1024, 1>(c.sws, vo, l0, sq[1]); dma<J * NT2 * 1024 + 2048, J * 1024, 1>(c.sws, vo, l0, sq[2]); \
    dma<J * NT2 * 1024 + 3072, J * 1024, 1>(c.sws, vo, l0, sq[3]); if (NT2 == 5) dma<J * NT2 * 1024 + 4096, J * 1024, 1>(c.sws, vo, l0, sq[4]); } while (0)
    setprio<3>();
    c.rdy |= (unsigned)__builtin_amdgcn_ballot_w64(mwatch == c.tag);
    const bool early = (c.rdy & 48u) == 48u;
    asm volatile("" ::: "memory");
    if (early) { PQB(0); PQB(1); if (QPRE == 3) PQB(2); }
#pragma unroll
    for (int m = 0; m < 4; ++m) {
      *(LAS u32x4*)(DMP + p * 10240 + m * 2048 + vo) = pk8(acc[m][0], acc[m][1]);
      *(LAS u32x4*)(DMP + p * 10240 + m * 2048 + 1024 + vo) = pk8(acc[m][2], acc[m][3]);
      if (m & 1) *(LAS u32x4*)(DMP + p * 10240 + 8192 + (m >> 1) * 1024 + vo) = pk8(acc[m - 1][4], acc[m][4]);
    }
    WAIT_LDS();
    BAR();
    STAMP(7);
    if (!early) { need(48u, true); PQB(0); PQB(1); if (QPRE == 3) PQB(2); }
    STAMP(8);
    if (QPRE == 2) PQB(2);
    waitv<2 * NT2>(); BAR();
    PQB(3);
    waitv<2 * NT2>(); BAR();
    PQB(4);
    f32x4 f0 = z4, f1 = z4;
    auto sum5 = [&](unsigned base, int n) {
      if (w < 2) {
        u32x4 t[8];
#pragma unroll
        for (int i = 0; i < n; ++i) t[i] = l128(base + i * 2560 + w * 1024 + vo);
#pragma unroll
        for (int i = 0; i < n; ++i) { f0 = mfma(E0, t[i], f0); f1 = mfma(E1, t[i], f1); }
      }
      if (w == 2) {
        u32x2 t[8];
#pragma unroll
        for (int i = 0; i < n; ++i) t[i] = *(LAS u32x2*)(base + i * 2560 + 2048 + (vo >> 1));
#pragma unroll
        for (int i = 0; i < n; ++i) f0 = mfma(E0, u32x4{t[i].x, t[i].y, 0u, 0u}, f0);
      }
    };
    waitv<2 * NT2>(); BAR();
    waitv<NT2>(); BAR();
    waitv<0>(); BAR();
    setprio<0>();
    if (NT2 == 5) { sum5(T5M, 8); PIN(f0); PIN(f1); }
    STAMP(9);
    BAR();
    if (NT2 == 5) {
      sum5(T5A, 4);
      const unsigned os = c.nt == 5 ? tl[4] * 16 * 1280 + cb * 160 : OOB;
      if (w < 2) st16<0>(pk8(f0, f1), ro, l15 * 1280 + g * 16 + w * 64, os);
      if (w == 2) __builtin_amdgcn_raw_buffer_store_b64(u32x2{pk2(f0[0], f0[1]), pk2(f0[2], f0[3])}, ro, l15 * 1280 + g * 8 + 128, os, 0);
    }
  }
  if (w >= 4) {
    u32x4 mf[25];
    const bool early = !any_nw(mwatch);
    auto ldm = [&](int lo, int hi) {
      vo = opq(vo);
#pragma unroll
      for (int i = lo; i < hi; ++i) { unsigned so = mb + (i / 5) * 5120 + ((i % 5) >> 2) * 4096; asm volatile("" : "+s"(so)); mf[i] = ld16(c.rws, vo + ((i % 5) & 3) * 1024, so); }
      SCHED0();
    };
    if (early) ldm(0, MPRE);
    WAIT_LDS();
    BAR();
    STAMP(7);
    u32x4 o0[5], o1[5];
    auto rd = [&](int j, u32x4 (&o)[5]) {
#pragma unroll
      for (int m = NT2 - 1; m >= 0; --m) {
        const unsigned ad = PQX + (p * 5 + j) * NT2 * 1024 + m * 1024;
        o[m] = j == 2 ? cat(*(LAS u32x2*)(ad + (vo >> 1)), *(LAS u32x2*)(ad + 512 + (vo >> 1))) : l128(ad + vo);
      }
    };
    {
      u32x4 t[10];
#pragma unroll
      for (int i = 0; i < 10; ++i) t[i] = l128(DMP + p * 10240 + i * 1024 + vo);
      SCHED0();
#pragma unroll
      for (int m = 0; m < 4; ++m) {
        acc[m][0] = mfma(E0, t[2 * m], acc[m][0]); acc[m][1] = mfma(E1, t[2 * m], acc[m][1]);
        acc[m][2] = mfma(E0, t[2 * m + 1], acc[m][2]); acc[m][3] = mfma(E1, t[2 * m + 1], acc[m][3]);
        acc[m][4] = mfma((m & 1) ? E1 : E0, t[8 + (m >> 1)], acc[m][4]);
      }
      SCHED0();
    }
    if (!early) { wait_words<PSL>(c, (lane < 5 ? lane : 4) * 5120 + 5116, mb); ldm(0, MPRE); }
    ldm(MPRE, 20);
    STAMP(8);
    auto chk = [&](int j) {
      auto bad = [&]() { unsigned m = 0;
#pragma unroll
        for (int n = 0; n < 5; ++n) m = acc_nw(m, mf[5 * j + n]);
        return any_nw(m); };
      RETRY(bad(), { _Pragma("unroll") for (int n = 0; n < 5; ++n) reld<0>(c, mf[5 * j + n], vo, mb + (5 * j + n) * 1024); });
    };
    auto mm = [&](int j, const u32x4 (&o)[5]) {
#pragma unroll
      for (int m = NT2 - 1; m >= 0; --m)
#pragma unroll
        for (int n = 0; n < 5; ++n) acc[m][n] = mfma(mf[5 * j + n], o[m], (m == 4 && j == 0) ? z4 : acc[m][n]);
      SCHED0();
    };
    chk(0); chk(1);
    BAR(); rd(0, o0);
    BAR(); rd(1, o1); SCHED0(); mm(0, o0);
    ldm(20, 25);
    chk(2);
    BAR(); rd(2, o0); SCHED0(); mm(1, o1);
    chk(3);
    BAR(); rd(3, o1); SCHED0(); mm(2, o0);
    chk(4);
    BAR(); rd(4, o0); SCHED0(); mm(3, o1);
    mm(4, o0);
    STAMP(9);
    if (NT2 == 5) put5(T5A + p * 2560, vo, acc[4]);
#pragma unroll
    for (int m = 3; m >= 1; --m) put5(FIN + ((p + m) & 3) * 7680 + (m - 1) * 2560, vo, acc[m]);
    WAIT_LDS();
    STAMP(10);
    BAR();
    u32x4 ta[3], tb_[3]; u32x2 tc[3];
#pragma unroll
    for (int i = 0; i < 3; ++i) { const unsigned d = FIN + p * 7680 + i * 2560; ta[i] = l128(d + vo); tb_[i] = l128(d + 1024 + vo); tc[i] = *(LAS u32x2*)(d + 2048 + (vo >> 1)); }
    SCHED0();
#pragma unroll
    for (int i = 0; i < 3; ++i) {
      acc[0][0] = mfma(E0, ta[i], acc[0][0]); acc[0][1] = mfma(E1, ta[i], acc[0][1]); acc[0][2] = mfma(E0, tb_[i], acc[0][2]); acc[0][3] = mfma(E1, tb_[i], acc[0][3]);
      acc[0][4] = mfma(E0, u32x4{tc[i].x, tc[i].y, 0u, 0u}, acc[0][4]);
    }
    const unsigned os = tl[0] * 16 * 1280 + cb * 160;
    st16<0>(pk8(acc[0][0], acc[0][1]), ro, l15 * 1280 + g * 16, os);
    st16<0>(pk8(acc[0][2], acc[0][3]), ro, l15 * 1280 + g * 16 + 64, os);
    __builtin_amdgcn_raw_buffer_store_b64(u32x2{pk2(acc[0][4][0], acc[0][4][1]), pk2(acc[0][4][2], acc[0][4][3])}, ro, l15 * 1280 + g * 8 + 128, os, 0);
  }
  STAMP(11);
  STAMP_FLUSH(w);
  __builtin_amdgcn_s_waitcnt(0);
  asm volatile("; end of role %0" ::"i"(ROLE) : "memory");
}

__device__ __forceinline__ const u16* kptr(unsigned lo, unsigned hi) { return (const u16*)(((uint64_t)hi << 32) | lo); }
__device__ __forceinline__ float ror_add(float v, int) { return v; }
template <int N> __device__ __forceinline__ float row_ror(float v) { return __builtin_bit_cast(float, __builtin_amdgcn_update_dpp(0, __builtin_bit_cast(int, v), 0x120 + N, 0xf, 0xf, true)); }

template <int type> __device__ __forceinline__ void role_gemm(Ctx& c, Plan pl, unsigned ga0, unsigned gb0, const u16* wo, const u16* out, int r0, int cc, float gs STAMP_ARG) {
  const int w = c.w;
  constexpr int WPRE = type == 1 ? 0 : 20;
  u32x4 wf[30], rf[5];
#pragma unroll
  for (int n = 0; n < 5; ++n) rf[n] = u32x4{0, 0, 0, 0};
  if constexpr (type == 0) {
    const int lane = fresh_lane(), g = lane >> 4, l15 = lane & 15, q4 = l15 >> 2, p4 = l15 & 3;
    const int wr = w >> 2, wc = w & 3;
    if (pl.rt) {
      waitv<13 + TW>();
      const unsigned ad = RT_LDS + (w - 4) * 5120 + (4 * g + q4) * 160;
#pragma unroll
      for (int n = 0; n < 5; ++n) { const unsigned co = n < 4 ? (n >> 1) * 64 + (n & 1) * 8 + p4 * 16 : 128 + p4 * 8; rf[n] = cat(ltr(ad + co), ltr(ad + co + 2560)); }
      WAIT_LDS();
    }
    f32x4 acc[5][4];
#pragma unroll
    for (int m = 0; m < 5; ++m)
#pragma unroll
      for (int n = 0; n < 4; ++n) acc[m][n] = f32x4{0.f, 0.f, 0.f, 0.f};
    const unsigned a0 = (wr * 80 + l15) * 128 + ((g ^ (l15 >> 1)) * 16);
    const int f = q4 | ((g & 1) << 2);
    const unsigned bb = 20480 + (8 * g + q4) * 256 + p4 * 8;
    const int t0 = (r0 < 12 ? 10 * r0 : 118) + wr * 5;
    unsigned oi[5];
    kloop<0, false, WPRE>(c, pl, ga0, gb0, a0, bb + (((2 * wc) ^ f) * 32), bb + (((2 * wc + 1) ^ f) * 32), acc, wf, rf, gs, []() {}, [&]() {
#pragma unroll
      for (int m = 0; m < 5; ++m) oi[m] = ld4(c.rws, (c.vo & 255) >> 2, G_OFF + 8192 + (t0 + m) * 64);
    } STAMP_PASS);
    RETRY(any_nw(umax3(umax3(oi[0], oi[1], oi[2]), oi[3], oi[4])), { _Pragma("unroll") for (int m = 0; m < 5; ++m) reld1(c, oi[m], (c.vo & 255) >> 2, G_OFF + 8192 + (t0 + m) * 64); });
    if (c.bid == 96 && w == 0) st4(c.tag, c.rws, c.vo ? OOB : 0, EP_OFF);
#pragma unroll
    for (int m = 0; m < 5; ++m) {
      RAMP(m);
      const float o_ = __builtin_bit_cast(float, oi[m]);
      const u32x4 o = {glu2(acc[m][0][0], acc[m][0][1], acc[m][2][0], acc[m][2][1], o_), glu2(acc[m][0][2], acc[m][0][3], acc[m][2][2], acc[m][2][3], o_),
                       glu2(acc[m][1][0], acc[m][1][1], acc[m][3][0], acc[m][3][1], o_), glu2(acc[m][1][2], acc[m][1][3], acc[m][3][2], acc[m][3][3], o_)};

      st16(o, c.rws, c.vo, t0 + m >= 10 * r0 ? HF_OFF + ((t0 + m) * 48 + 4 * cc + wc) * 1024 : OOB);

      SCHED0();
    }
    STAMP(27);
    waitv<0>();
    STAMP(29);
    BAR();
    st4(c.tag, c.rws, c.vo ? OOB : (r0 * 12 + cc) * 64, (w || r0 == 13) ? OOB : FH_OFF);
#pragma unroll
    for (int i = WPRE; i < 30; ++i) wf[i] = ldwd(c, i);
    STAMP(3);
  } else {
    const int wr = w & 3, wc = w >> 2;
    unsigned a0, bx, by;
    {
      const int lane = fresh_lane(), g = lane >> 4, l15 = lane & 15, q4 = l15 >> 2, p4 = l15 & 3;
      pl.va = plan_va(w, lane); plan_vt(pl.vb, w, lane, type == 1); c.vo = lane * 16;
      a0 = (wr * 64 + l15) * 128 + ((g ^ (l15 >> 1)) * 16);
      const unsigned bb = 32768 + (8 * g + q4) * 320 + p4 * 8;
      bx = bb + (((5 * wc) ^ (g & 1)) * 32); by = bb + (((5 * wc + 1) ^ (g & 1)) * 32);
    }
    f32x4 acc[4][5];
    if constexpr (type == 1) {
#pragma unroll
      for (int m = 0; m < 4; ++m)
#pragma unroll
        for (int n = 0; n < 5; ++n) acc[m][n] = f32x4{EPS, EPS, EPS, EPS};
      const int t0 = r0 * 16 + wr * 4;
      unsigned gv[4];
      kloop<1, false, WPRE>(c, pl, ga0, gb0, a0, bx, by, acc, wf, rf, 0.f, []() {}, []() {} STAMP_PASS);
      const int hq = 2 * cc + wc;
      u32x4 f0[4], f1[4]; u32x2 f2[4];
#pragma unroll
      for (int m = 0; m < 4; ++m) {
        RAMP(m);
        f0[m] = phi8(acc[m][0], acc[m][1]); PIN(f0[m]); PIN(acc[m][0]); PIN(acc[m][1]); SCHED0();
        f1[m] = phi8(acc[m][2], acc[m][3]); PIN(f1[m]); PIN(acc[m][2]); PIN(acc[m][3]); SCHED0();
        if (m & 1) { u32x4 hh = phi8(acc[m - 1][4], acc[m][4]); PIN(hh); PIN(acc[m - 1][4]); PIN(acc[m][4]); f2[m - 1] = u32x2{hh.x, hh.y}; f2[m] = u32x2{hh.z, hh.w}; SCHED0(); }
      }
      STAMP(21);
      const int lane = fresh_lane();
      const unsigned zv = (lane & 7) * 8 * KVS + (lane >> 4) * 16 + ((lane & 8) ? 192 : 0), zs = KV_OFF + hq * KVS + 13 * 1024;
#pragma unroll
      for (int m = 0; m < 4; ++m) gv[m] = ld4(c.rws, (c.vo & 255) >> 2, G_OFF + (t0 + m) * 64);
      RETRY(any_nw(umax3(umax3(gv[0], gv[1], gv[2]), gv[3], 0u)), { _Pragma("unroll") for (int m = 0; m < 4; ++m) reld1(c, gv[m], (c.vo & 255) >> 2, G_OFF + (t0 + m) * 64); });
#pragma unroll
      for (int m = 0; m < 4; ++m)
#pragma unroll
        for (int n = 0; n < 5; ++n) { acc[m][n] *= __builtin_bit_cast(float, gv[m]); PIN(acc[m][n]); }
      u32x4 a0_, a1_, a2_;
      __builtin_amdgcn_s_sleep(Z_DELAY);
      STAMP(24);
      {
        int spin = 0;
        const unsigned zv2 = (lane & 8) ? zv + 64 : zv + 128;
#pragma nounroll
        for (;;) {
          asm volatile("buffer_load_dwordx4 %0, %3, %5, %6 offen sc1\n\tbuffer_load_dwordx4 %1, %3, %5, %6 offen offset:64 sc1\n\tbuffer_load_dwordx4 %2, %4, %5, %6 offen sc1\n\ts_waitcnt vmcnt(0)"
                       : "=&v"(a0_), "=&v"(a1_), "=&v"(a2_) : "v"(zv), "v"(zv2), "s"(c.sws), "s"(zs) : "memory");
          if (!any_nw(acc_nw(acc_nw(acc_nw(0u, a0_), a1_), a2_)) || c.dead) break;
          __builtin_amdgcn_s_sleep(3);
          if (++spin > SPIN_CAP) c.dead = true;
        }
      }
      STAMP(22);
      setprio<3>();
      float zz[12];
#pragma unroll
      for (int i = 0; i < 4; ++i) { const unsigned x0 = a0_[i], x1 = a1_[i], x2 = a2_[i]; zz[i] = __builtin_bit_cast(float, x0); zz[4 + i] = __builtin_bit_cast(float, x1); zz[8 + i] = __builtin_bit_cast(float, x2); }
#pragma unroll
      for (int i = 0; i < 12; ++i) zz[i] = dadd<0xB1>(zz[i]);
#pragma unroll
      for (int i = 0; i < 12; ++i) zz[i] = dadd<0x4E>(zz[i]);
#pragma unroll
      for (int i = 0; i < 12; ++i) zz[i] = dadd<0x141>(zz[i]);
      unsigned zp[6], zn[10];
#pragma unroll
      for (int i = 0; i < 6; ++i) zp[i] = pk2(zz[2 * i], zz[2 * i + 1]);
#pragma unroll
      for (int i = 0; i < 6; ++i) zn[i] = dpp<0x128, 0xc>(zp[i], zp[i]);
#pragma unroll
      for (int i = 0; i < 4; ++i) zn[6 + i] = dpp<0x128, 0x3>(zp[i], zp[i]);
      const u32x4 z00 = {zkeep<0, 0>() ? zn[0] : 0u, zkeep<0, 0>() ? zn[1] : 0u, zkeep<0, 16>() ? zn[2] : 0u, zkeep<0, 16>() ? zn[3] : 0u};
      const u32x4 z01 = {zkeep<0, 32>() ? zn[4] : 0u, zkeep<0, 32>() ? zn[5] : 0u, zkeep<0, 48>() ? zn[6] : 0u, zkeep<0, 48>() ? zn[7] : 0u};
      const u32x4 z02 = {zkeep<0, 64>() ? zn[8] : 0u, zkeep<0, 64>() ? zn[9] : 0u, 0u, 0u};
      const u32x4 z11 = {0u, 0u, zkeep<1, 48>() ? zn[6] : 0u, zkeep<1, 48>() ? zn[7] : 0u};
      const u32x4 z12 = {zkeep<1, 64>() ? zn[8] : 0u, zkeep<1, 64>() ? zn[9] : 0u, 0u, 0u};
      f32x4 d0[4], d1[4];
      const f32x4 e4 = {EPS, EPS, EPS, EPS};
#pragma unroll
      for (int m = 0; m < 4; ++m) { d0[m] = mfma(z00, f0[m], e4); d1[m] = mfma(z11, f1[m], e4); }
#pragma unroll
      for (int m = 0; m < 4; ++m) { d0[m] = mfma(z01, f1[m], d0[m]); d1[m] = mfma(z12, u32x4{f2[m].x, f2[m].y, 0u, 0u}, d1[m]); }
#pragma unroll
      for (int m = 0; m < 4; ++m) d0[m] = mfma(z02, u32x4{f2[m].x, f2[m].y, 0u, 0u}, d0[m]);
      SCHED0();
      STAMP(23);
#pragma unroll
      for (int m = 0; m < 4; ++m) {
        RAMP(m);
        f32x4 o[5];
#pragma unroll
        for (int n = 0; n < 5; ++n) o[n] = acc[m][n] * __builtin_amdgcn_rcpf(n < 4 ? d0[m][n] : d1[m][0]);
        const unsigned so = PF_OFF + ((t0 + m) * 20 + 5 * cc) * 1024;
        if (wc == 0) { st16(pk8(o[0], o[1]), c.rws, c.vo, so); st16(pk8(o[2], o[3]), c.rws, c.vo, so + 1024); st8(u32x2{pk2(o[4][0], o[4][1]), pk2(o[4][2], o[4][3])}, c.rws, c.vo >> 1, so + 2048); }
        else { st8(u32x2{pk2(o[0][0], o[0][1]), pk2(o[0][2], o[0][3])}, c.rws, c.vo >> 1, so + 2560); st16(pk8(o[1], o[2]), c.rws, c.vo, so + 3072); st16(pk8(o[3], o[4]), c.rws, c.vo, so + 4096); }
        SCHED0();
      }
      STAMP(27);
      waitv<0>();
      STAMP(29);
      BAR();
      st4(c.tag, c.rws, c.vo ? OOB : (156 + r0 * 4 + cc) * 64, w ? OOB : FH_OFF);
      STAMP(3);
      STAMP(11);
      STAMP_FLUSH(w);
    } else {
      const float e2 = wc ? 0.f : EPS;
#pragma unroll
      for (int m = 0; m < 4; ++m) { acc[m][0] = acc[m][1] = f32x4{EPS, EPS, EPS, EPS}; acc[m][2] = f32x4{e2, e2, e2, e2}; acc[m][3] = acc[m][4] = f32x4{0.f, 0.f, 0.f, 0.f}; }
      unsigned vw[2];
#pragma unroll
      for (int i = 0; i < 2; ++i) { const unsigned u = (w + 8 * i) * 64 + (c.vo >> 4), r = (u * 6554u) >> 16; vw[i] = __builtin_elementwise_min(cc * 80u + r, 639u) * 1280 + (u - 10 * r) * 16; PIN(vw[i]); }
      kloop<1, true, WPRE>(c, pl, ga0, gb0, a0, bx, by, acc, wf, rf, 0.f, []() {}, []() {} STAMP_PASS);
      {
        const u32x4 so_ = mksrd(wo, DM * DM * 2);
        dma<0, 0>(so_, vw[0], WO_LDS + w * 1024, r0 * 160u);
        if (w < 7) dma<8192, 0>(so_, vw[1], WO_LDS + w * 1024, r0 * 160u);
      }
      {
        auto fphi = [&](int kb, int n) { SCHED0(); return phi8(acc[2 * kb][n], acc[2 * kb + 1][n]); };
        auto fid = [&](int kb, int n) { return u32x4{pk2(acc[2 * kb][n][0], acc[2 * kb][n][1]), pk2(acc[2 * kb][n][2], acc[2 * kb][n][3]),
                                                      pk2(acc[2 * kb + 1][n][0], acc[2 * kb + 1][n][1]), pk2(acc[2 * kb + 1][n][2], acc[2 * kb + 1][n][3])}; };
        const unsigned fk = FX + wr * 2048 + wc * 3 * 8192 + c.vo, fv = FX + wr * 2048 + (wc ? 8 : 5) * 8192 + c.vo;
#pragma unroll
        for (int kb = 0; kb < 2; ++kb) { *(LAS u32x4*)(fv + kb * 1024) = fid(kb, 3); *(LAS u32x4*)(fv + 8192 + kb * 1024) = fid(kb, 4); }
        if (wc) { *(LAS u32x4*)(fv - 8192) = fid(0, 2); *(LAS u32x4*)(fv - 8192 + 1024) = fid(1, 2); }
        else { *(LAS u32x4*)(fk + 16384) = fphi(0, 2); *(LAS u32x4*)(fk + 16384 + 1024) = fphi(1, 2); }
#pragma unroll
        for (int kb = 0; kb < 2; ++kb) { *(LAS u32x4*)(fk + kb * 1024) = fphi(kb, 0); *(LAS u32x4*)(fk + 8192 + kb * 1024) = fphi(kb, 1); }
      }
      STAMP(27);
      WAIT_LDS();
      STAMP(28);
      BAR();
      STAMP(29);
      const f32x4 z4 = {0.f, 0.f, 0.f, 0.f};
      {
        const int i = (w + 1) >> 1, full = (w & 1) | (w == 0), ja = full ? (i ? i - 1 : 0) : i + 1, jb = full ? ja + 1 : ja;
        const u32x4 ones = {0x3F803F80u, 0x3F803F80u, 0x3F803F80u, 0x3F803F80u};
        const unsigned ak = FX + i * 8192 + c.vo, aa = FX + (5 + ja) * 8192 + c.vo, ab = FX + (5 + jb) * 8192 + c.vo;
        f32x4 ta = z4, tb = z4, tz = z4;
        u32x4 pk[8], va_[8], vb_[8];
        const unsigned kb0 = KV_OFF + (r0 * 8 + cc) * KVS;
        if (full) setprio<3>();
#pragma unroll
        for (int kb = 0; kb < 8; ++kb) pk[kb] = l128(ak + kb * 1024);
#pragma unroll
        for (int kb = 0; kb < 8; ++kb) va_[kb] = l128(aa + kb * 1024);
        SCHED0();
#pragma unroll
        for (int kb = 0; kb < 8; ++kb) tz = mfma(ones, pk[kb], tz);
        st4(__builtin_bit_cast(unsigned, tz[0]), c.rws, c.vo < 256 ? c.vo >> 2 : OOB, full ? kb0 + 13 * 1024 + i * 64 : OOB);
        SCHED0();
        setprio<0>();
#pragma unroll
        for (int kb = 0; kb < 8; ++kb) vb_[kb] = l128(ab + kb * 1024);
#pragma unroll
        for (int kb = 0; kb < 8; ++kb) ta = mfma(va_[kb], pk[kb], ta);
#pragma unroll
        for (int kb = 0; kb < 8; ++kb) tb = mfma(vb_[kb], pk[kb], tb);
        st16(__builtin_bit_cast(u32x4, ta), c.rws, c.vo, kb0 + (2 * i + ja) * 1024);
        st16(__builtin_bit_cast(u32x4, tb), c.rws, c.vo, full ? kb0 + (2 * i + jb) * 1024 : OOB);
      }
      waitv<3>();
      BAR();
      STAMP(3);
      if (w == 4) setprio<1>();
      if (w >= 5) {
#pragma unroll
        for (int i = WPRE; i < 30; ++i) wf[i] = ldwd(c, i);
      }
      if (w < 5) {
        const int lane = fresh_lane(), g = lane >> 4, l15 = lane & 15, q4 = l15 >> 2, p4 = l15 & 3;
        const int hd = ((16 * w + l15) * 205) >> 12, hlo = (16 * w * 205) >> 12, hhi = ((16 * w + 15) * 205) >> 12;
        const int gc = g < 2 ? g : 2, ro0 = 5 * hd + 2 * gc, ro1 = ro0 + 1;
        const unsigned pv0 = (2 * w + (ro0 >> 2)) * 1024 + (l15 + 16 * (ro0 & 3)) * 16;
        const unsigned pv1 = g < 2 ? (2 * w + (ro1 >> 2)) * 1024 + (l15 + 16 * (ro1 & 3)) * 16 : pv0;
        const unsigned kb = KV_OFF + cc * KVS;
        u32x4 bw[2][5];
#pragma unroll
        for (int hh = 0; hh < 2; ++hh)
#pragma unroll
          for (int n = 0; n < 5; ++n) {
            const unsigned ad = WO_LDS + (20 * (hh ? hhi : hlo) + 8 * g + q4) * 160 + (n < 4 ? (n >> 1) * 64 + (n & 1) * 8 + p4 * 16 : 128 + p4 * 8);
            bw[hh][n] = cat(ltr(ad), ltr(ad + 640));
          }
        const int T = 5 * cc + w, blk = __builtin_amdgcn_readfirstlane(r0 * 20 + (T >> 1)), half = T & 1;
        unsigned mv = lane * 16 + half * 8;
        unsigned k0 = hd == hlo && g < 3 ? ~0u : 0u, k1 = hd == hlo && g < 2 ? ~0u : 0u, k2 = hd == hhi && hhi != hlo && g < 3 ? ~0u : 0u, k3 = hd == hhi && hhi != hlo && g < 2 ? ~0u : 0u;
        PIN(mv); PIN(k0); PIN(k1); PIN(k2); PIN(k3);
        SCHED0();
        waitv<0>();
        STAMP(30);
        u32x4 p0[8], p1[8];
#pragma unroll
        for (int r = 0; r < 8; ++r) { p0[r] = ld16(c.rws, pv0, kb + r * 8 * KVS); p1[r] = ld16(c.rws, pv1, kb + r * 8 * KVS); }
#pragma unroll
        for (int i = WPRE; i < 30; ++i) wf[i] = ldwd(c, i);
        const unsigned fv = ld4(c.rws, flag_off(c, lane), FH_OFF);
        SCHED0();
        auto bad = [&]() { unsigned m = 0;
#pragma unroll
          for (int r = 0; r < 8; ++r) m = acc_nw(acc_nw(m, p0[r]), p1[r]);
          return any_nw(m); };
        RETRY(bad(), { _Pragma("unroll") for (int r = 0; r < 8; ++r) {
          asm volatile("buffer_load_dwordx4 %0, %2, %4, %5 offen sc1\n\tbuffer_load_dwordx4 %1, %3, %4, %5 offen sc1" : "+v"(p0[r]), "+v"(p1[r]) : "v"(pv0), "v"(pv1), "s"(c.sws), "s"(kb + r * 8 * KVS) : "memory"); }
          asm volatile("s_waitcnt vmcnt(0)" : "+v"(p0[0]), "+v"(p1[0]), "+v"(p0[1]), "+v"(p1[1]), "+v"(p0[2]), "+v"(p1[2]), "+v"(p0[3]), "+v"(p1[3]), "+v"(p0[4]), "+v"(p1[4]), "+v"(p0[5]), "+v"(p1[5]), "+v"(p0[6]), "+v"(p1[6]), "+v"(p0[7]), "+v"(p1[7]) :: "memory"); });
        f32x4 s0 = __builtin_bit_cast(f32x4, p0[0]), s1 = __builtin_bit_cast(f32x4, p1[0]);
#pragma unroll
        for (int r = 1; r < 8; ++r) { s0 += __builtin_bit_cast(f32x4, p0[r]); s1 += __builtin_bit_cast(f32x4, p1[r]); }
        PIN(s0); PIN(s1);
        STAMP(31);
        const u32x2 lo2 = {pk2(s0[0], s0[1]), pk2(s0[2], s0[3])}, hi2 = {pk2(s1[0], s1[1]), pk2(s1[2], s1[3])};
        const u32x4 alo = {lo2.x & k0, lo2.y & k0, hi2.x & k1, hi2.y & k1}, ahi = {lo2.x & k2, lo2.y & k2, hi2.x & k3, hi2.y & k3};
        f32x4 mt[5];
#pragma unroll
        for (int n = 0; n < 5; ++n) mt[n] = mfma(alo, bw[0][n], z4);
#pragma unroll
        for (int n = 0; n < 5; ++n) mt[n] = mfma(ahi, bw[1][n], mt[n]);
#pragma unroll
        for (int n = 0; n < 5; ++n) st8(u32x2{pk2(mt[n][0], mt[n][1]), pk2(mt[n][2], mt[n][3])}, c.rws, mv, MF_OFF + (blk * 5 + n) * 1024);
        STAMP(26);
        c.rdy = (unsigned)__builtin_amdgcn_ballot_w64(opq(fv) == c.tag);
      } else {
        waitv<0>(); __builtin_amdgcn_s_sleep(20);
        c.rdy = (unsigned)__builtin_amdgcn_ballot_w64(ld4(c.rws, flag_off(c, fresh_lane()), FH_OFF) == c.tag);
      }
      BAR();
    }
  }
  if constexpr (type != 1) phase2<type, (type == 0 ? 5 : 4)>(c, wf, out STAMP_PASS);
}

struct WgTab { unsigned v[256][8]; };
constexpr WgTab make_tab() {
  WgTab t{};
  for (int bid = 0; bid < 256; ++bid) {
    const int xcd = bid & 7, idx = bid >> 3, xi = xcd >> 2, xj = xcd & 3;
    int r0 = 0, cc = 0; unsigned ga = 0, gb = 0;
    if (idx < 8) { r0 = 4 * xi + (idx >> 1); cc = 2 * xj + (idx & 1); ga = r0 * 256u * 1280u; gb = 1280u + cc * 160u; }
    else if (idx < 12) { r0 = 4 * xi + idx - 8; cc = xj; ga = r0 * 256u * 1280u; gb = cc * 320u; }
    else if (bid >= 252) { r0 = 13; cc = xj; ga = 1888u * 1280u; gb = cc * 256u; }
    else {
      const int i = idx - 12;
      if (i < 18) { r0 = 7 * xi + i / 3; cc = 3 * xj + i % 3; } else { r0 = 6; cc = 3 * xj + (xi ? 2 : i - 18); }
      ga = (r0 < 12 ? r0 * 160u : 1888u) * 1280u; gb = cc * 256u;
    }
    t.v[bid][0] = r0; t.v[bid][1] = cc; t.v[bid][2] = ga; t.v[bid][3] = gb;
    if (idx < 8 || idx >= 12) {
      const int cut[8] = {0, 5, 10, 14, 18, 23, 28, 32}, side = xcd & 1;
      int want = idx < 8 ? idx : idx - 4, n = 0;
      for (int pass = 4; pass <= 5; ++pass)
        for (int cb = 0; cb < 8; ++cb)
          for (int sg = 0; sg < 7; ++sg) {
            const bool mine = side == 0 ? sg < (cb < 4 ? 4 : 3) : sg >= (cb < 4 ? 4 : 3);
            if (mine && cut[sg + 1] - cut[sg] == pass) { if (n == want) { t.v[bid][4] = 32 * (xcd >> 1) + cut[sg]; t.v[bid][5] = pass; t.v[bid][6] = sg; t.v[bid][7] = cb; } ++n; }
          }
    }
  }
  return t;
}
static __constant__ const WgTab wg_tab = make_tab();
typedef unsigned u32x8 __attribute__((ext_vector_type(8)));

__global__ __launch_bounds__(512, 2) void k_fused(const u16* x_, const u16* wqkv_, const u16* wo_, const u16* wg_, const u16* wu_, const u16* wd_, const u16* gw_, char* ws_, u16* out_
) {
  u32x16 ka; u32x2 kb; u32x8 tb;
  asm volatile("s_load_dwordx16 %0, %2, 0x0\n\ts_load_dwordx2 %1, %2, 0x40" : "=&s"(ka), "=&s"(kb) : "s"(__builtin_amdgcn_kernarg_segment_ptr()));
  asm volatile("s_load_dwordx8 %0, %1, %2" : "=&s"(tb) : "s"((uint64_t)(uintptr_t)&wg_tab), "s"(blockIdx.x * 32));
  SCHED0();
  const int w = __builtin_amdgcn_readfirstlane(threadIdx.x >> 6);
  const int bid = blockIdx.x, idx = bid >> 3;
  const bool isT = bid < 96, isQ = isT && bid >= 64;
  const int lane = __builtin_amdgcn_mbcnt_hi(~0u, __builtin_amdgcn_mbcnt_lo(~0u, 0u)), g = lane >> 4, l15 = lane & 15;
  Plan pl;
  pl.lo = w < 4; pl.a2 = isT || pl.lo; pl.a3 = isT; pl.b2 = !isT || pl.lo; pl.b3 = !isT;
  pl.va = plan_va(w, lane); pl.la = w * 1024;
  if (isT) {
    plan_vt(pl.vb, w, lane, isQ);
    pl.lb[0] = 32768 + w * 1024; pl.lb[1] = 32768 + (w + 8) * 1024; pl.lb[2] = 32768 + (16 + (w & 3)) * 1024; pl.lb[3] = 0;
  } else {
    const int bt = w & 1, p0 = 2 * bt + ((w >> 1) & 1);
    const unsigned vm = (unsigned)(4 * p0 + g) * 3072u + (((l15 >> 1) ^ (g | (bt << 2))) * 32) + (l15 & 1) * 16, lm = 20480 + (w >> 2) * 16384 + p0 * 1024;
#pragma unroll
    for (int i = 0; i < 4; ++i) { pl.vb[i] = vm + i * 16 * 3072; pl.lb[i] = lm + i * 4096; }
  }
  asm volatile("s_waitcnt lgkmcnt(0)" : "+s"(ka), "+s"(kb), "+s"(tb), "+v"(pl.va), "+v"(pl.vb[0]), "+v"(pl.vb[1]), "+v"(pl.vb[2]), "+v"(pl.vb[3]));
  const int r0 = tb[0], cc = tb[1];
  const unsigned ga0 = tb[2], gb0 = tb[3], gbs = isT ? 64u * 3840u : 64u * 3072u;
  const bool rt = bid >= 128 && w >= 4 && w < 7;
  pl.rt = rt;
  STAMP(12);
  unsigned epoch;
  asm volatile("s_load_dword %0, %1, %2" : "=&s"(epoch) : "s"(((uint64_t)ka[15] << 32) | ka[14]), "s"(EP_OFF));
  Ctx c;
  c.rws = mkrsrc(kptr(ka[14], ka[15]), WS_USED); c.sws = u32x4{ka[14], ka[15] & 0xffffu, WS_USED, 0x00020000u};
  c.bid = bid; c.w = w; c.vo = lane * 16; c.tb = tb[4]; c.nt = tb[5]; c.cb = tb[7]; c.dead = false; c.rdy = 0;
  c.rot = ((c.cb >> 2) ^ tb[6]) & 1;

  const u16* out = kptr(kb[0], kb[1]);
  pl.sa = u32x4{ka[0], ka[1] & 0xffffu, SEQ * DM * 2, 0x00020000u};
  pl.sb = u32x4{isT ? ka[2] : pl.lo ? ka[6] : ka[8], (isT ? ka[3] : pl.lo ? ka[7] : ka[9]) & 0xffffu, isT ? DM * 1920 * 2u : DM * FF * 2u, 0x00020000u};
  if (rt) {
    const u32x4 sd = {ka[10], ka[11] & 0xffffu, FF * DM * 2, 0x00020000u};
    const unsigned u = (bid - 128) * 3 + w - 4, gd = (u >> 3) * 32u * 1280u + (u & 7) * 160u, ld = RT_LDS + (w - 4) * 5120;
    unsigned vd[5];
#pragma unroll
    for (int i = 0; i < 5; ++i) { const unsigned q = 64 * i + lane, r = (q * 6554u) >> 16; vd[i] = r * 1120u + q * 16; }
    dma<0, 0>(sd, vd[0], ld, gd); dma<1024, 0>(sd, vd[1], ld, gd); dma<2048, 0>(sd, vd[2], ld, gd); dma<3072, 0>(sd, vd[3], ld, gd); dma<4096, 0>(sd, vd[4], ld, gd);
  }
  pl.issue(0, ga0, gb0);
  {
    uint64_t pc; asm volatile("s_getpc_b64 %0" : "=s"(pc));
    const u32x4 sc = {(unsigned)pc & ~255u, (unsigned)(pc >> 32) & 0xffffu,
CODE_BYTES,
 0x00020000u};
    asm volatile("s_mov_b32 m0, %2\n\ts_nop 0\n\tbuffer_load_dword %0, %1, 0 offen lds" ::"v"(((idx * 8 + w) * 2 + (lane & 1)) * 128), "s"(sc), "s"(SPARE + w * 256));
  }
  const bool gt = bid >= 96 && bid < 224 && w < 4;
  const unsigned gl = 2 * STG + w * 7168;
  if (gt) {
    const u32x4 sg = {ka[12], ka[13] & 0xffffu, DM * 2, 0x00020000u};
    const unsigned gg = ((bid - 96) * 16 + w * 4) * 1280;
    dma<0, 0>(pl.sa, c.vo, gl, gg); dma<1024, 1024>(pl.sa, c.vo, gl, gg); dma<2048, 2048>(pl.sa, c.vo, gl, gg); dma<3072, 3072>(pl.sa, c.vo, gl, gg); dma<4096, 4096>(pl.sa, c.vo, gl, gg);
    dma<5120, 0>(sg, c.vo, gl, 0u); dma<6144, 1024>(sg, c.vo, gl, 0u);
  }

  __builtin_amdgcn_s_barrier();
  pl.issue(STG, ga0 + 128, gb0 + gbs);
  {
    const u32x4 st = {ka[2], ka[3] & 0xffffu, isQ ? DM * 1920 * 2u : 0u, 0x00020000u};
    asm volatile("s_mov_b32 m0, %3\n\ts_nop 0\n\tbuffer_load_dword %0, %1, %2 offen lds" ::"v"(c.vo * 8), "s"(st), "s"(128u * 3840u + ((bid & 31) * 8 + w) * 7680u), "s"(SPARE + w * 256));
  }
  STAMP(13);
  asm volatile("s_waitcnt lgkmcnt(0)" : "+s"(epoch));
  c.tag = epoch * 0x9E3779B1u + 0x7F4A7C15u;
  float gs = 0.f;
  if (gt) {
    waitv<7 + TW>();
#pragma unroll
    for (int i = 0; i < 5; ++i) {
      const u32x4 xa = l128(gl + g * 1280 + l15 * 16 + i * 256), ga_ = l128(gl + 5120 + l15 * 16 + i * 256);
#pragma unroll
      for (int j = 0; j < 4; ++j) { const unsigned a = xa[j], b = ga_[j]; gs = __builtin_amdgcn_fdot2_f32_bf16(__builtin_bit_cast(bf16x2_t, a), __builtin_bit_cast(bf16x2_t, b), gs, false); }
    }
    gs += row_ror<8>(gs); gs += row_ror<4>(gs); gs += row_ror<2>(gs); gs += row_ror<1>(gs);
    const float e = __builtin_amdgcn_exp2f(__builtin_fminf((lane & 1) ? gs * LOG2E : -gs * LOG2E, 100.f));
    gs = (lane & 1) ? 1.f + e : __builtin_amdgcn_rcpf(1.f + e);
  }
  if (!isT) role_gemm<0>(c, pl, ga0, gb0, nullptr, out, r0, cc, gs STAMP_PASS);
  else if (!isQ) role_gemm<2>(c, pl, ga0, gb0, kptr(ka[4], ka[5]), out, r0, cc, gs STAMP_PASS);
  else role_gemm<1>(c, pl, ga0, gb0, nullptr, out, r0, cc, gs STAMP_PASS);
}

extern "C" void kernel_launch(void* const* d_in, const int* in_sizes, int n_in, void* d_out, int out_size, void* d_ws, size_t ws_size, hipStream_t stream) {
  (void)hipFuncSetAttribute((const void*)k_fused, hipFuncAttributeMaxDynamicSharedMemorySize, LDS_BYTES);
  k_fused<<<256, 512, LDS_BYTES, stream>>>((const u16*)d_in[0], (const u16*)d_in[1], (const u16*)d_in[2], (const u16*)d_in[4], (const u16*)d_in[5], (const u16*)d_in[6],
                                           (const u16*)d_in[7], (char*)d_ws, (u16*)d_out
  );
}
```
